```python
import torch

def _softmax(x, axis=-1):
    return torch.softmax(x, dim=axis)
def _gelu(x):
    return torch.nn.functional.gelu(x, approximate="tanh")
def _silu(x):
    return torch.nn.functional.silu(x)
def _sigmoid(x):
    return torch.sigmoid(x)
def _layernorm(x, axis=-1, eps=1e-5):
    m = x.mean(dim=axis, keepdim=True)
    v = x.var(dim=axis, keepdim=True, unbiased=False)
    return (x - m) / torch.sqrt(v + eps)
def _scatter_add(tgt, idx, src, axis=0):
    return tgt.clone().index_add_(axis, idx, src)
def _segment_sum(src, seg, n):
    out = torch.zeros((n,) + src.shape[1:], dtype=src.dtype)
    return out.index_add_(0, seg, src)
def _pad(x, pw):
    flat = tuple(v for pair in reversed(pw) for v in pair)
    return torch.nn.functional.pad(x, flat)

kernel_name = 'standalone__dense_activation_gelu__prompt'

def setup_inputs():

    _w = (lambda a: (lambda t: t.to(torch.bfloat16) if t.is_floating_point() else t)(torch.as_tensor(a)))
    B, S, D, F = 1, 2048, 448, 1792
    return {
        "x": _w((lambda *s: torch.randn(*s, dtype=torch.bfloat16) * 0.05)(B, S, D)),
        "w1": _w((lambda *s: torch.randn(*s, dtype=torch.bfloat16) * 3.2)(D, F) * (D ** -0.5)),
        "b1": _w((lambda *s: torch.randn(*s, dtype=torch.bfloat16) * 3.2)(F) * 0.01),
        "w2": _w((lambda *s: torch.randn(*s, dtype=torch.bfloat16) * 0.05)(F, D) * (F ** -0.5)),
        "b2": _w((lambda *s: torch.randn(*s, dtype=torch.bfloat16) * 0.1)(D) * 0.01),
    }

def reference(x, w1, b1, w2, b2):
    h = torch.einsum("bsd,df->bsf", x, w1) + b1
    h = _gelu(h)
    return torch.einsum("bsf,fd->bsd", h, w2) + b2
```

```cpp
#include <hip/hip_runtime.h>
#include <hip/hip_bf16.h>
typedef short bf16x8 __attribute__((ext_vector_type(8)));
typedef float f32x16 __attribute__((ext_vector_type(16)));
constexpr int M=2048, D=448, F=1792;

__device__ __forceinline__ float bf2f(unsigned short b){ return __uint_as_float((unsigned)b<<16); }
__device__ __forceinline__ unsigned short f2bf(float f){ __hip_bfloat16 h = __float2bfloat16(f); return *(unsigned short*)&h; }
__device__ __forceinline__ float gelu(float x){
  float u = 0.7978845608028654f*(x+0.044715f*x*x*x);
  return 0.5f*x*(1.f+tanhf(u));
}
template<int K,int N,bool ACT>
__global__ __launch_bounds__(64) void gemm_naive(const unsigned short* A,const unsigned short* B,const unsigned short* bias,unsigned short* C){
  int lane=threadIdx.x, r=lane&31, h=lane>>5;
  int m0=blockIdx.y*32, n0=blockIdx.x*32;
  f32x16 acc; for(int i=0;i<16;++i) acc[i]=0.f;
  for(int k0=0;k0<K;k0+=16){
    bf16x8 a=*(const bf16x8*)(A+(size_t)(m0+r)*K+k0+8*h);
    bf16x8 b; for(int j=0;j<8;++j) b[j]=(short)B[(size_t)(k0+8*h+j)*N+n0+r];
    acc=__builtin_amdgcn_mfma_f32_32x32x16_bf16(a,b,acc,0,0,0);
  }
  float bv=bf2f(bias[n0+r]);
  for(int i=0;i<16;++i){
    int row=(i&3)+8*(i>>2)+4*h;
    float v=acc[i]+bv; if(ACT) v=gelu(v);
    C[(size_t)(m0+row)*N+n0+r]=f2bf(v);
  }
}
extern "C" void kernel_launch(void* const* d_in, const int* in_sizes, int n_in,
                              void* d_out, int out_size, void* d_ws, size_t ws_size,
                              hipStream_t stream) {
  const unsigned short* x=(const unsigned short*)d_in[0];
  const unsigned short* w1=(const unsigned short*)d_in[1];
  const unsigned short* b1=(const unsigned short*)d_in[2];
  const unsigned short* w2=(const unsigned short*)d_in[3];
  const unsigned short* b2=(const unsigned short*)d_in[4];
  unsigned short* h=(unsigned short*)d_ws;
  gemm_naive<D,F,true><<<dim3(F/32,M/32),64,0,stream>>>(x,w1,b1,h);
  gemm_naive<F,D,false><<<dim3(D/32,M/32),64,0,stream>>>(h,w2,b2,(unsigned short*)d_out);
}
```

```cpp
#include <hip/hip_runtime.h>
#include <stdint.h>
typedef unsigned short u16;
static __device__ inline float b2f(u16 v) { return __uint_as_float(((uint32_t)v) << 16); }
static __device__ inline u16 f2b(float f) { uint32_t u = __float_as_uint(f); u += 0x7fff + ((u >> 16) & 1); return (u16)(u >> 16); }
__global__ __launch_bounds__(512) void naive(const u16* x, const u16* w1, const u16* b1, const u16* w2, const u16* b2, u16* out) {
  uint64_t t0 = __builtin_amdgcn_s_memrealtime();
  __shared__ float xs[8 * 448];
  __shared__ float hs[8 * 1792 + 23040];
  int r0 = blockIdx.x * 8;
  for (int i = threadIdx.x; i < 8 * 448; i += 512) xs[i] = b2f(x[r0 * 448 + i]);
  __syncthreads();
  for (int f = threadIdx.x; f < 1792; f += 512) {
    float acc[8];
    for (int r = 0; r < 8; r++) acc[r] = 0.f;
    for (int k = 0; k < 448; k++) {
      float w = b2f(w1[k * 1792 + f]);
      for (int r = 0; r < 8; r++) acc[r] += xs[r * 448 + k] * w;
    }
    float b = b2f(b1[f]);
    for (int r = 0; r < 8; r++) {
      float v = b2f(f2b(acc[r] + b));
      float g = 0.5f * v * (1.f + tanhf(0.7978845608f * (v + 0.044715f * v * v * v)));
      hs[r * 1792 + f] = b2f(f2b(g));
    }
  }
  __syncthreads();
  for (int d = threadIdx.x; d < 448; d += 512) {
    float acc[8];
    for (int r = 0; r < 8; r++) acc[r] = 0.f;
    for (int k = 0; k < 1792; k++) {
      float w = b2f(w2[k * 448 + d]);
      for (int r = 0; r < 8; r++) acc[r] += hs[r * 1792 + k] * w;
    }
    float b = b2f(b2[d]);
    for (int r = 0; r < 8; r++) __hip_atomic_store(&out[(r0 + r) * 448 + d], f2b(acc[r] + b), __ATOMIC_RELAXED, __HIP_MEMORY_SCOPE_AGENT);
  }
  while (__builtin_amdgcn_s_memrealtime() - t0 < 150000ull) __builtin_amdgcn_s_sleep(1);
}
extern "C" void kernel_launch(void* const* d_in, const int* in_sizes, int n_in, void* d_out, int out_size, void* d_ws, size_t ws_size, hipStream_t stream) {
  naive<<<256, 512, 0, stream>>>((const u16*)d_in[0], (const u16*)d_in[1], (const u16*)d_in[2], (const u16*)d_in[3], (const u16*)d_in[4], (u16*)d_out);
}
```

```cpp
#include <hip/hip_runtime.h>
typedef unsigned short u16;

__global__ __launch_bounds__(512, 2) void fused_mlp(const u16* x, const u16* w1, const u16* b1, const u16* w2,
                                                   const u16* b2, u16* out, char* ws) {
  asm volatile(
      "s_load_dwordx8 s[8:15], %0, 0x0\n"
      "s_load_dwordx4 s[16:19], %0, 0x20\n"
      "s_load_dwordx2 s[36:37], %0, 0x30\n"
      "s_cmp_ge_u32 %1, 224\n"
      "s_cbranch_scc1 LW_%=\n"
      "v_readfirstlane_b32 s40, %2\n"
      "v_and_b32 v2, 63, %2\n"
      "s_lshr_b32 s49, %1, 3\n"
      "s_mul_i32 s42, s49, 37\n"
      "s_lshr_b32 s42, s42, 8\n"
      "s_mul_i32 s43, s42, 7\n"
      "s_sub_u32 s43, s49, s43\n"
      "s_bfe_u32 s44, %1, 0x10002\n"
      "s_and_b32 s45, %1, 3\n"
      "s_lshl_b32 s45, s45, 2\n"
      "s_add_u32 s45, s45, s42\n"
      "s_mul_i32 s46, s44, 7\n"
      "s_add_u32 s46, s46, s43\n"
      "s_lshr_b32 s40, s40, 6\n"
      "s_and_b32 s47, s40, 3\n"
      "s_mov_b32 s38, 8916992\n"
      "s_mov_b32 s39, 0x20000\n"
      "s_cmp_ge_u32 s40, 4\n"
      "s_cbranch_scc1 LC_%=\n"
      "s_setprio 1\n"
      "v_lshrrev_b32 v24, 3, v2\n"
      "v_mul_u32_u24 v12, 896, v24\n"
      "v_lshrrev_b32 v5, 4, v2\n"
      "v_xor_b32 v4, v5, v2\n"
      "v_and_b32 v4, 7, v4\n"
      "v_lshl_add_u32 v4, v4, 4, v12\n"
      "v_xor_b32 v5, 64, v4\n"
      "v_add_u32 v5, 6144, v5\n"
      "v_add_u32 v6, 12288, v4\n"
      "v_add_u32 v7, 12288, v5\n"
      "v_bfe_u32 v25, v2, 4, 1\n"
      "v_lshlrev_b32 v25, 2, v25\n"
      "v_xor_b32 v25, v25, v2\n"
      "v_and_b32 v25, 7, v25\n"
      "v_lshlrev_b32 v25, 4, v25\n"
      "v_mul_u32_u24 v8, 3584, v24\n"
      "v_add_u32 v8, v8, v25\n"
      "v_add_u32 v9, 27648, v8\n"
      "s_lshl_b32 s59, s45, 2\n"
      "s_add_u32 s59, s59, s47\n"
      "s_mul_i32 s59, s59, 28672\n"
      "s_mul_i32 s60, s47, 57344\n"
      "s_lshl_b32 s49, s46, 8\n"
      "s_add_u32 s60, s60, s49\n"
      "s_lshl_b32 s62, s47, 12\n"
      "s_lshl_b32 s61, s47, 11\n"
      "s_mul_i32 s48, s43, 1\n"
      "s_and_b32 s50, %1, 3\n"
      "s_mul_i32 s50, s50, 2\n"
      "s_add_u32 s48, s48, s50\n"
      "s_mul_i32 s50, s48, 37\n"
      "s_lshr_b32 s50, s50, 8\n"
      "s_mul_i32 s50, s50, 7\n"
      "s_sub_u32 s48, s48, s50\n"
      "s_lshl_b32 s50, s48, 7\n"
      "s_add_u32 s59, s59, s50\n"
      "s_mul_i32 s51, s48, 229376\n"
      "s_add_u32 s60, s60, s51\n"
      "s_mov_b32 s22, 1605632\n"
      "s_mov_b32 s23, 0x20000\n"
      "s_mov_b32 s26, 1835008\n"
      "s_mov_b32 s27, 0x20000\n"
      "s_mov_b32 s30, 1605632\n"
      "s_mov_b32 s31, 0x20000\n"
      "s_waitcnt lgkmcnt(0)\n"
      "s_mov_b32 s24, s8\n"
      "s_and_b32 s25, s9, 0xffff\n"
      "s_mov_b32 s28, s10\n"
      "s_and_b32 s29, s11, 0xffff\n"
      "s_mov_b32 s20, s14\n"
      "s_and_b32 s21, s15, 0xffff\n"
      "s_add_u32 s63, s60, 0\n"
      "s_add_u32 m0, s61, 114688\n"
      "s_nop 0\n"
      "buffer_load_dwordx4 v8, s[28:31], s63 offen lds\n"
      "buffer_load_dwordx4 v9, s[28:31], s63 offen offset:1024 lds\n"
      "s_add_u32 m0, s62, 0\n"
      "s_nop 0\n"
      "buffer_load_dwordx4 v4, s[24:27], s59 offen lds\n"
      "buffer_load_dwordx4 v5, s[24:27], s59 offen offset:1024 lds\n"
      "buffer_load_dwordx4 v6, s[24:27], s59 offen offset:2048 lds\n"
      "buffer_load_dwordx4 v7, s[24:27], s59 offen offset:3072 lds\n"
      "s_sub_u32 s50, s59, 896\n"
      "s_sub_u32 s51, s60, 1605632\n"
      "s_cmp_ge_u32 s48, 6\n"
      "s_cselect_b32 s63, s51, s60\n"
      "s_cselect_b32 s54, s50, s59\n"
      "s_add_u32 s63, s63, 229376\n"
      "s_add_u32 m0, s61, 122880\n"
      "s_nop 0\n"
      "buffer_load_dwordx4 v8, s[28:31], s63 offen lds\n"
      "buffer_load_dwordx4 v9, s[28:31], s63 offen offset:1024 lds\n"
      "s_add_u32 s63, s54, 128\n"
      "s_add_u32 m0, s62, 16384\n"
      "s_nop 0\n"
      "buffer_load_dwordx4 v4, s[24:27], s63 offen lds\n"
      "buffer_load_dwordx4 v5, s[24:27], s63 offen offset:1024 lds\n"
      "buffer_load_dwordx4 v6, s[24:27], s63 offen offset:2048 lds\n"
      "buffer_load_dwordx4 v7, s[24:27], s63 offen offset:3072 lds\n"
      "s_cmp_ge_u32 s48, 5\n"
      "s_cselect_b32 s63, s51, s60\n"
      "s_cselect_b32 s54, s50, s59\n"
      "s_add_u32 s63, s63, 458752\n"
      "s_add_u32 m0, s61, 131072\n"
      "s_nop 0\n"
      "buffer_load_dwordx4 v8, s[28:31], s63 offen lds\n"
      "buffer_load_dwordx4 v9, s[28:31], s63 offen offset:1024 lds\n"
      "s_add_u32 s63, s54, 256\n"
      "s_add_u32 m0, s62, 32768\n"
      "s_nop 0\n"
      "buffer_load_dwordx4 v4, s[24:27], s63 offen lds\n"
      "buffer_load_dwordx4 v5, s[24:27], s63 offen offset:1024 lds\n"
      "buffer_load_dwordx4 v6, s[24:27], s63 offen offset:2048 lds\n"
      "buffer_load_dwordx4 v7, s[24:27], s63 offen offset:3072 lds\n"
      "v_add_u32 v12, v12, v25\n"
      "v_add_u32 v13, 6144, v12\n"
      "v_add_u32 v14, 12288, v12\n"
      "v_add_u32 v15, 18432, v12\n"
      "s_and_b32 s41, %1, 3\n"
      "s_bfe_u32 s7, %1, 0x10002\n"
      "s_mul_i32 s7, s7, 4\n"
      "s_add_u32 s41, s41, s7\n"
      "s_mul_i32 s7, s41, 37\n"
      "s_lshr_b32 s7, s7, 8\n"
      "s_mul_i32 s7, s7, 7\n"
      "s_sub_u32 s41, s41, s7\n"
      "s_lshr_b32 s58, s47, 1\n"
      "s_lshl1_add_u32 s58, s58, s47\n"
      "s_mul_i32 s58, s58, 28672\n"
      "s_lshl_b32 s49, s43, 7\n"
      "s_add_u32 s58, s58, s49\n"
      "s_add_u32 s57, s58, 1605632\n"
      "s_mul_i32 s56, s41, 229376\n"
      "s_add_u32 s64, s58, s56\n"
      "s_add_u32 s65, s64, 229376\n"
      "s_cmp_ge_u32 s65, s57\n"
      "s_cselect_b32 s56, 1605632, 0\n"
      "s_sub_u32 s65, s65, s56\n"
      "s_add_u32 s66, s65, 229376\n"
      "s_cmp_ge_u32 s66, s57\n"
      "s_cselect_b32 s56, 1605632, 0\n"
      "s_sub_u32 s66, s66, s56\n"
      "s_add_u32 s67, s66, 229376\n"
      "s_cmp_ge_u32 s67, s57\n"
      "s_cselect_b32 s56, 1605632, 0\n"
      "s_sub_u32 s67, s67, s56\n"
      "s_add_u32 s68, s67, 229376\n"
      "s_cmp_ge_u32 s68, s57\n"
      "s_cselect_b32 s56, 1605632, 0\n"
      "s_sub_u32 s68, s68, s56\n"
      "s_add_u32 s69, s68, 229376\n"
      "s_cmp_ge_u32 s69, s57\n"
      "s_cselect_b32 s56, 1605632, 0\n"
      "s_sub_u32 s69, s69, s56\n"
      "s_add_u32 s70, s69, 229376\n"
      "s_cmp_ge_u32 s70, s57\n"
      "s_cselect_b32 s56, 1605632, 0\n"
      "s_sub_u32 s70, s70, s56\n"
      "s_and_b32 s37, s37, 0xffff\n"
      "s_lshl_b32 s52, s45, 7\n"
      "s_add_u32 s52, s52, 8912896\n"
      "s_waitcnt vmcnt(12)\n"
      "s_barrier\n"
      "s_cmp_ge_u32 s48, 4\n"
      "s_cselect_b32 s63, s51, s60\n"
      "s_cselect_b32 s54, s50, s59\n"
      "s_add_u32 s63, s63, 688128\n"
      "s_add_u32 m0, s61, 139264\n"
      "s_nop 0\n"
      "buffer_load_dwordx4 v8, s[28:31], s63 offen lds\n"
      "buffer_load_dwordx4 v9, s[28:31], s63 offen offset:1024 lds\n"
      "s_add_u32 s63, s54, 384\n"
      "s_add_u32 m0, s62, 49152\n"
      "s_nop 0\n"
      "buffer_load_dwordx4 v4, s[24:27], s63 offen lds\n"
      "buffer_load_dwordx4 v5, s[24:27], s63 offen offset:1024 lds\n"
      "buffer_load_dwordx4 v6, s[24:27], s63 offen offset:2048 lds\n"
      "buffer_load_dwordx4 v7, s[24:27], s63 offen offset:3072 lds\n"
      "s_cmp_ge_u32 s48, 3\n"
      "s_cselect_b32 s63, s51, s60\n"
      "s_cselect_b32 s54, s50, s59\n"
      "s_add_u32 s63, s63, 917504\n"
      "s_add_u32 m0, s61, 147456\n"
      "s_nop 0\n"
      "buffer_load_dwordx4 v8, s[28:31], s63 offen lds\n"
      "buffer_load_dwordx4 v9, s[28:31], s63 offen offset:1024 lds\n"
      "s_add_u32 s63, s54, 512\n"
      "s_add_u32 m0, s62, 65536\n"
      "s_nop 0\n"
      "buffer_load_dwordx4 v4, s[24:27], s63 offen lds\n"
      "buffer_load_dwordx4 v5, s[24:27], s63 offen offset:1024 lds\n"
      "buffer_load_dwordx4 v6, s[24:27], s63 offen offset:2048 lds\n"
      "buffer_load_dwordx4 v7, s[24:27], s63 offen offset:3072 lds\n"
      "s_waitcnt vmcnt(18)\n"
      "s_barrier\n"
      "s_cmp_ge_u32 s48, 2\n"
      "s_cselect_b32 s63, s51, s60\n"
      "s_cselect_b32 s54, s50, s59\n"
      "s_add_u32 s63, s63, 1146880\n"
      "s_add_u32 m0, s61, 155648\n"
      "s_nop 0\n"
      "buffer_load_dwordx4 v8, s[28:31], s63 offen lds\n"
      "buffer_load_dwordx4 v9, s[28:31], s63 offen offset:1024 lds\n"
      "s_add_u32 s63, s54, 640\n"
      "s_add_u32 m0, s62, 81920\n"
      "s_nop 0\n"
      "buffer_load_dwordx4 v4, s[24:27], s63 offen lds\n"
      "buffer_load_dwordx4 v5, s[24:27], s63 offen offset:1024 lds\n"
      "buffer_load_dwordx4 v6, s[24:27], s63 offen offset:2048 lds\n"
      "buffer_load_dwordx4 v7, s[24:27], s63 offen offset:3072 lds\n"
      "s_waitcnt vmcnt(18)\n"
      "s_barrier\n"
      "s_cmp_ge_u32 s48, 1\n"
      "s_cselect_b32 s63, s51, s60\n"
      "s_cselect_b32 s54, s50, s59\n"
      "s_add_u32 s63, s63, 1376256\n"
      "s_add_u32 m0, s61, 114688\n"
      "s_nop 0\n"
      "buffer_load_dwordx4 v8, s[28:31], s63 offen lds\n"
      "buffer_load_dwordx4 v9, s[28:31], s63 offen offset:1024 lds\n"
      "s_add_u32 s63, s54, 768\n"
      "s_add_u32 m0, s62, 98304\n"
      "s_nop 0\n"
      "buffer_load_dwordx4 v4, s[24:27], s63 offen lds\n"
      "buffer_load_dwordx4 v5, s[24:27], s63 offen offset:1024 lds\n"
      "buffer_load_dwordx4 v6, s[24:27], s63 offen offset:2048 lds\n"
      "buffer_load_dwordx4 v7, s[24:27], s63 offen offset:3072 lds\n"
      "s_cmp_ge_u32 s48, 3\n"
      "s_cselect_b32 s63, s51, s60\n"
      "s_add_u32 s63, s63, 917632\n"
      "s_add_u32 m0, s61, 0\n"
      "s_nop 0\n"
      "buffer_load_dwordx4 v8, s[28:31], s63 offen lds\n"
      "buffer_load_dwordx4 v9, s[28:31], s63 offen offset:1024 lds\n"
      "s_waitcnt vmcnt(20)\n"
      "s_barrier\n"
      "buffer_load_dwordx2 v[16:17], off, s[36:39], s52 sc1\n"
      "s_cmp_ge_u32 s48, 2\n"
      "s_cselect_b32 s63, s51, s60\n"
      "s_add_u32 s63, s63, 1147008\n"
      "s_add_u32 m0, s61, 8192\n"
      "s_nop 0\n"
      "buffer_load_dwordx4 v8, s[28:31], s63 offen lds\n"
      "buffer_load_dwordx4 v9, s[28:31], s63 offen offset:1024 lds\n"
      "s_cmp_ge_u32 s48, 1\n"
      "s_cselect_b32 s63, s51, s60\n"
      "s_add_u32 s63, s63, 1376384\n"
      "s_add_u32 m0, s61, 16384\n"
      "s_nop 0\n"
      "buffer_load_dwordx4 v8, s[28:31], s63 offen lds\n"
      "buffer_load_dwordx4 v9, s[28:31], s63 offen offset:1024 lds\n"
      "s_waitcnt vmcnt(19)\n"
      "s_barrier\n"
      "s_add_u32 s63, s60, 128\n"
      "s_add_u32 m0, s61, 24576\n"
      "s_nop 0\n"
      "buffer_load_dwordx4 v8, s[28:31], s63 offen lds\n"
      "buffer_load_dwordx4 v9, s[28:31], s63 offen offset:1024 lds\n"
      "s_cmp_ge_u32 s48, 6\n"
      "s_cselect_b32 s63, s51, s60\n"
      "s_add_u32 s63, s63, 229504\n"
      "s_add_u32 m0, s61, 32768\n"
      "s_nop 0\n"
      "buffer_load_dwordx4 v8, s[28:31], s63 offen lds\n"
      "buffer_load_dwordx4 v9, s[28:31], s63 offen offset:1024 lds\n"
      "s_waitcnt vmcnt(17)\n"
      "s_barrier\n"
      "s_cmp_ge_u32 s48, 5\n"
      "s_cselect_b32 s63, s51, s60\n"
      "s_add_u32 s63, s63, 458880\n"
      "s_add_u32 m0, s61, 40960\n"
      "s_nop 0\n"
      "buffer_load_dwordx4 v8, s[28:31], s63 offen lds\n"
      "buffer_load_dwordx4 v9, s[28:31], s63 offen offset:1024 lds\n"
      "s_cmp_ge_u32 s48, 4\n"
      "s_cselect_b32 s63, s51, s60\n"
      "s_add_u32 s63, s63, 688256\n"
      "s_add_u32 m0, s61, 49152\n"
      "s_nop 0\n"
      "buffer_load_dwordx4 v8, s[28:31], s63 offen lds\n"
      "buffer_load_dwordx4 v9, s[28:31], s63 offen offset:1024 lds\n"
      "s_waitcnt vmcnt(15)\n"
      "s_barrier\n"
      "s_add_u32 m0, s62, 131072\n"
      "s_nop 0\n"
      "buffer_load_dwordx4 v12, s[20:23], s64 offen lds\n"
      "buffer_load_dwordx4 v13, s[20:23], s64 offen offset:1024 lds\n"
      "buffer_load_dwordx4 v14, s[20:23], s64 offen offset:2048 lds\n"
      "buffer_load_dwordx4 v15, s[20:23], s64 offen offset:3072 lds\n"
      "s_waitcnt vmcnt(12)\n"
      "s_barrier\n"
      "s_add_u32 m0, s62, 65536\n"
      "s_nop 0\n"
      "buffer_load_dwordx4 v12, s[20:23], s65 offen lds\n"
      "buffer_load_dwordx4 v13, s[20:23], s65 offen offset:1024 lds\n"
      "buffer_load_dwordx4 v14, s[20:23], s65 offen offset:2048 lds\n"
      "buffer_load_dwordx4 v15, s[20:23], s65 offen offset:3072 lds\n"
      "s_add_u32 m0, s62, 81920\n"
      "s_nop 0\n"
      "buffer_load_dwordx4 v12, s[20:23], s66 offen lds\n"
      "buffer_load_dwordx4 v13, s[20:23], s66 offen offset:1024 lds\n"
      "buffer_load_dwordx4 v14, s[20:23], s66 offen offset:2048 lds\n"
      "buffer_load_dwordx4 v15, s[20:23], s66 offen offset:3072 lds\n"
      "s_waitcnt vmcnt(12)\n"
      "s_barrier\n"
      "s_add_u32 m0, s62, 147456\n"
      "s_nop 0\n"
      "buffer_load_dwordx4 v12, s[20:23], s67 offen lds\n"
      "buffer_load_dwordx4 v13, s[20:23], s67 offen offset:1024 lds\n"
      "buffer_load_dwordx4 v14, s[20:23], s67 offen offset:2048 lds\n"
      "buffer_load_dwordx4 v15, s[20:23], s67 offen offset:3072 lds\n"
      "s_add_u32 m0, s62, 98304\n"
      "s_nop 0\n"
      "buffer_load_dwordx4 v12, s[20:23], s68 offen lds\n"
      "buffer_load_dwordx4 v13, s[20:23], s68 offen offset:1024 lds\n"
      "buffer_load_dwordx4 v14, s[20:23], s68 offen offset:2048 lds\n"
      "buffer_load_dwordx4 v15, s[20:23], s68 offen offset:3072 lds\n"
      "s_add_u32 m0, s62, 114688\n"
      "s_nop 0\n"
      "buffer_load_dwordx4 v12, s[20:23], s69 offen lds\n"
      "buffer_load_dwordx4 v13, s[20:23], s69 offen offset:1024 lds\n"
      "buffer_load_dwordx4 v14, s[20:23], s69 offen offset:2048 lds\n"
      "buffer_load_dwordx4 v15, s[20:23], s69 offen offset:3072 lds\n"
      "s_barrier\n"
      "s_add_u32 m0, s62, 0\n"
      "s_nop 0\n"
      "buffer_load_dwordx4 v12, s[20:23], s70 offen lds\n"
      "buffer_load_dwordx4 v13, s[20:23], s70 offen offset:1024 lds\n"
      "buffer_load_dwordx4 v14, s[20:23], s70 offen offset:2048 lds\n"
      "buffer_load_dwordx4 v15, s[20:23], s70 offen offset:3072 lds\n"
      "s_add_u32 s63, s64, 57344\n"
      "s_add_u32 m0, s62, 16384\n"
      "s_nop 0\n"
      "buffer_load_dwordx4 v12, s[20:23], s63 offen lds\n"
      "buffer_load_dwordx4 v13, s[20:23], s63 offen offset:1024 lds\n"
      "buffer_load_dwordx4 v14, s[20:23], s63 offen offset:2048 lds\n"
      "buffer_load_dwordx4 v15, s[20:23], s63 offen offset:3072 lds\n"
      "s_barrier\n"
      "s_waitcnt vmcnt(16)\n"
      "s_barrier\n"
      "s_add_u32 s63, s65, 57344\n"
      "s_add_u32 m0, s62, 32768\n"
      "s_nop 0\n"
      "buffer_load_dwordx4 v12, s[20:23], s63 offen lds\n"
      "buffer_load_dwordx4 v13, s[20:23], s63 offen offset:1024 lds\n"
      "buffer_load_dwordx4 v14, s[20:23], s63 offen offset:2048 lds\n"
      "buffer_load_dwordx4 v15, s[20:23], s63 offen offset:3072 lds\n"
      "s_add_u32 s63, s66, 57344\n"
      "s_add_u32 m0, s62, 49152\n"
      "s_nop 0\n"
      "buffer_load_dwordx4 v12, s[20:23], s63 offen lds\n"
      "buffer_load_dwordx4 v13, s[20:23], s63 offen offset:1024 lds\n"
      "buffer_load_dwordx4 v14, s[20:23], s63 offen offset:2048 lds\n"
      "buffer_load_dwordx4 v15, s[20:23], s63 offen offset:3072 lds\n"
      "s_barrier\n"
      "s_add_u32 s63, s67, 57344\n"
      "s_add_u32 m0, s62, 131072\n"
      "s_nop 0\n"
      "buffer_load_dwordx4 v12, s[20:23], s63 offen lds\n"
      "buffer_load_dwordx4 v13, s[20:23], s63 offen offset:1024 lds\n"
      "buffer_load_dwordx4 v14, s[20:23], s63 offen offset:2048 lds\n"
      "buffer_load_dwordx4 v15, s[20:23], s63 offen offset:3072 lds\n"
      "s_add_u32 s63, s68, 57344\n"
      "s_add_u32 m0, s62, 65536\n"
      "s_nop 0\n"
      "buffer_load_dwordx4 v12, s[20:23], s63 offen lds\n"
      "buffer_load_dwordx4 v13, s[20:23], s63 offen offset:1024 lds\n"
      "buffer_load_dwordx4 v14, s[20:23], s63 offen offset:2048 lds\n"
      "buffer_load_dwordx4 v15, s[20:23], s63 offen offset:3072 lds\n"
      "s_add_u32 s63, s69, 57344\n"
      "s_add_u32 m0, s62, 81920\n"
      "s_nop 0\n"
      "buffer_load_dwordx4 v12, s[20:23], s63 offen lds\n"
      "buffer_load_dwordx4 v13, s[20:23], s63 offen offset:1024 lds\n"
      "buffer_load_dwordx4 v14, s[20:23], s63 offen offset:2048 lds\n"
      "buffer_load_dwordx4 v15, s[20:23], s63 offen offset:3072 lds\n"
      "s_add_u32 s63, s70, 57344\n"
      "s_add_u32 m0, s62, 147456\n"
      "s_nop 0\n"
      "buffer_load_dwordx4 v12, s[20:23], s63 offen lds\n"
      "buffer_load_dwordx4 v13, s[20:23], s63 offen offset:1024 lds\n"
      "buffer_load_dwordx4 v14, s[20:23], s63 offen offset:2048 lds\n"
      "buffer_load_dwordx4 v15, s[20:23], s63 offen offset:3072 lds\n"
      "s_cmp_eq_u32 s47, 2\n"
      "s_cbranch_scc1 LPO_%=\n"
      "s_waitcnt vmcnt(16)\n"
      "s_barrier\n"
      "s_waitcnt vmcnt(0)\n"
      "s_barrier\n"
      "s_branch LEND_%=\n"
      "LPO_%=:\n"
      "s_lshl_b32 s53, s45, 2\n"
      "s_add_u32 s53, s53, s44\n"
      "s_lshl_b32 s53, s53, 12\n"
      "s_add_u32 s53, s53, 8388608\n"
      "v_and_b32 v18, 31, v2\n"
      "v_min_u32 v18, 27, v18\n"
      "v_lshrrev_b32 v19, 5, v2\n"
      "v_lshlrev_b32 v19, 13, v19\n"
      "v_lshl_add_u32 v18, v18, 7, v19\n"
      "s_mov_b32 s55, 0x4000\n"
      "v_add_u32 v16, 0x9E3779B9, v16\n"
      "v_add_u32 v17, 0x7F4A7C15, v17\n"
      "s_mov_b32 exec_hi, 0\n"
      "LP1_%=:\n"
      "buffer_load_dwordx2 v[20:21], v18, s[36:39], s53 offen sc1\n"
      "s_waitcnt vmcnt(0)\n"
      "v_cmp_ne_u64 vcc, v[20:21], v[16:17]\n"
      "s_cmp_eq_u32 vcc_lo, 0\n"
      "s_cbranch_scc1 LP1d_%=\n"
      "s_sleep 2\n"
      "s_sub_u32 s55, s55, 1\n"
      "s_cbranch_scc0 LP1_%=\n"
      "LP1d_%=:\n"
      "s_barrier\n"
      "s_mov_b64 exec, -1\n"
      "s_barrier\n"
      "s_cmp_lg_u32 s46, 0\n"
      "s_cbranch_scc1 LEND_%=\n"
      "s_mov_b64 exec, 1\n"
      "buffer_store_dwordx2 v[16:17], off, s[36:39], s52 sc1\n"
      "s_mov_b64 exec, -1\n"
      "s_branch LEND_%=\n"
      "LC_%=:\n"
      "s_and_b32 s48, s47, 1\n"
      "s_setprio 2\n"
      "s_lshl1_add_u32 s20, s46, s48\n"
      "s_lshl_b32 s21, s46, 8\n"
      "s_lshr_b32 s23, s47, 1\n"
      "s_lshl1_add_u32 s22, s43, s23\n"
      "s_lshl_b32 s22, s22, 6\n"
      "s_and_b32 s41, %1, 3\n"
      "s_bfe_u32 s49, %1, 0x10002\n"
      "s_mul_i32 s49, s49, 4\n"
      "s_add_u32 s41, s41, s49\n"
      "s_mul_i32 s49, s41, 37\n"
      "s_lshr_b32 s49, s49, 8\n"
      "s_mul_i32 s49, s49, 7\n"
      "s_sub_u32 s41, s41, s49\n"
      "s_lshl2_add_u32 s50, s45, s23\n"
      "s_lshl_b32 s50, s50, 12\n"
      "s_lshr_b32 s49, s20, 0\n"
      "s_lshl_b32 s49, s49, 7\n"
      "s_add_u32 s50, s50, s49\n"
      "s_add_u32 s50, s50, 8388608\n"
      "s_waitcnt lgkmcnt(0)\n"
      "s_add_u32 s12, s12, s21\n"
      "s_addc_u32 s13, s13, 0\n"
      "s_load_dwordx16 s[52:67], s[12:13], 0x0\n"
      "s_load_dwordx8 s[24:31], s[12:13], 0x80\n"
      "s_load_dwordx8 s[68:75], s[12:13], 0xa0\n"
      "s_load_dwordx16 s[80:95], s[12:13], 0x40\n"
      "s_load_dwordx4 s[96:99], s[12:13], 0xe0\n"
      "s_load_dwordx4 s[4:7], s[12:13], 0xf0\n"
      "s_load_dwordx8 s[8:15], s[12:13], 0xc0\n"
      "s_and_b32 s37, s37, 0xffff\n"
      "v_and_b32 v202, 31, v2\n"
      "v_lshrrev_b32 v203, 5, v2\n"
      "v_lshl_add_u32 v200, v203, 3, s22\n"
      "global_load_dwordx2 v[248:249], v200, s[16:17]\n"
      "global_load_dwordx2 v[250:251], v200, s[16:17] offset:16\n"
      "global_load_dwordx2 v[252:253], v200, s[16:17] offset:32\n"
      "global_load_dwordx2 v[254:255], v200, s[16:17] offset:48\n"
      "v_bfe_u32 v200, v2, 1, 3\n"
      "v_xor_b32 v200, v200, v203\n"
      "s_lshl_b32 s49, s47, 12\n"
      "v_lshl_add_u32 v201, v202, 7, s49\n"
      "v_xor_b32 v16, 0, v200\n"
      "v_lshl_add_u32 v4, v16, 4, v201\n"
      "v_xor_b32 v16, 2, v200\n"
      "v_lshl_add_u32 v5, v16, 4, v201\n"
      "v_xor_b32 v16, 4, v200\n"
      "v_lshl_add_u32 v6, v16, 4, v201\n"
      "v_xor_b32 v16, 6, v200\n"
      "v_lshl_add_u32 v7, v16, 4, v201\n"
      "v_bfe_u32 v200, v2, 4, 1\n"
      "v_lshlrev_b32 v200, 1, v200\n"
      "v_bfe_u32 v201, v2, 1, 1\n"
      "v_or_b32 v200, v200, v201\n"
      "v_bfe_u32 v201, v2, 3, 1\n"
      "v_lshl_or_b32 v200, v201, 2, v200\n"
      "v_lshlrev_b32 v200, 4, v200\n"
      "v_and_b32 v201, 1, v2\n"
      "v_lshl_or_b32 v200, v201, 3, v200\n"
      "v_bfe_u32 v201, v2, 2, 2\n"
      "v_lshl_add_u32 v200, v201, 7, v200\n"
      "s_lshl_b32 s49, s48, 6\n"
      "v_lshl_add_u32 v8, v203, 10, v200\n"
      "v_add_u32 v9, 0, v8\n"
      "v_add_u32 v8, 114688, v8\n"
      "v_xor_b32 v17, 64, v8\n"
      "v_xor_b32 v19, 64, v9\n"
      "v_lshl_add_u32 v10, v203, 9, v200\n"
      "s_lshl_b32 s49, s23, 6\n"
      "v_xor_b32 v10, s49, v10\n"
      "s_lshl_b32 s49, s23, 13\n"
      "v_add_u32 v10, s49, v10\n"
      "v_xor_b32 v11, 64, v10\n"
      "v_add_u32 v12, 65536, v10\n"
      "v_add_u32 v13, 65536, v11\n"
      "v_add_u32 v14, 131072, v10\n"
      "v_add_u32 v15, 131072, v11\n"
      "s_lshl1_add_u32 s49, s45, s44\n"
      "s_lshl1_add_u32 s49, s49, s48\n"
      "s_mul_i32 s51, s49, 28672\n"
      "s_add_u32 s51, s51, s22\n"
      "v_mul_u32_u24 v3, 896, v202\n"
      "v_lshl_add_u32 v3, v203, 4, v3\n"
      "v_add_u32 v3, s51, v3\n"
      "s_mul_i32 s21, s49, 56\n"
      "s_lshl_b32 s51, s23, 2\n"
      "s_add_u32 s21, s21, s51\n"
      "s_lshl_b32 s21, s21, 11\n"
      "s_lshl2_add_u32 s49, s45, s47\n"
      "s_mul_i32 s49, s49, 56\n"
      "s_lshl2_add_u32 s20, s46, s49\n"
      "s_lshl_b32 s20, s20, 11\n"
      "v_lshlrev_b32 v16, 4, v2\n"
      "v_mov_b32 v20, 0xc0135761\n"
      "v_mov_b32 v21, 0xc0135761\n"
      "v_mov_b32 v18, 0xffff0000\n"
      "s_lshl_b32 s23, s45, 7\n"
      "s_add_u32 s23, s23, 8912896\n"
      "s_waitcnt lgkmcnt(0)\n"
      "v_lshlrev_b32_e64 v24, 16, s52\n"
      "v_and_b32 v25, s52, v18\n"
      "v_lshlrev_b32_e64 v26, 16, s53\n"
      "v_and_b32 v27, s53, v18\n"
      "v_lshlrev_b32_e64 v28, 16, s56\n"
      "v_and_b32 v29, s56, v18\n"
      "v_lshlrev_b32_e64 v30, 16, s57\n"
      "v_and_b32 v31, s57, v18\n"
      "v_lshlrev_b32_e64 v32, 16, s60\n"
      "v_and_b32 v33, s60, v18\n"
      "v_lshlrev_b32_e64 v34, 16, s61\n"
      "v_and_b32 v35, s61, v18\n"
      "v_lshlrev_b32_e64 v36, 16, s64\n"
      "v_and_b32 v37, s64, v18\n"
      "v_lshlrev_b32_e64 v38, 16, s65\n"
      "v_and_b32 v39, s65, v18\n"
      "v_lshlrev_b32_e64 v40, 16, s80\n"
      "v_and_b32 v41, s80, v18\n"
      "v_lshlrev_b32_e64 v42, 16, s81\n"
      "v_and_b32 v43, s81, v18\n"
      "v_lshlrev_b32_e64 v44, 16, s84\n"
      "v_and_b32 v45, s84, v18\n"
      "v_lshlrev_b32_e64 v46, 16, s85\n"
      "v_and_b32 v47, s85, v18\n"
      "v_lshlrev_b32_e64 v48, 16, s88\n"
      "v_and_b32 v49, s88, v18\n"
      "v_lshlrev_b32_e64 v50, 16, s89\n"
      "v_and_b32 v51, s89, v18\n"
      "v_lshlrev_b32_e64 v52, 16, s92\n"
      "v_and_b32 v53, s92, v18\n"
      "v_lshlrev_b32_e64 v54, 16, s93\n"
      "v_and_b32 v55, s93, v18\n"
      "v_lshlrev_b32_e64 v56, 16, s24\n"
      "v_and_b32 v57, s24, v18\n"
      "v_lshlrev_b32_e64 v58, 16, s25\n"
      "v_and_b32 v59, s25, v18\n"
      "v_lshlrev_b32_e64 v60, 16, s28\n"
      "v_and_b32 v61, s28, v18\n"
      "v_lshlrev_b32_e64 v62, 16, s29\n"
      "v_and_b32 v63, s29, v18\n"
      "v_lshlrev_b32_e64 v64, 16, s68\n"
      "v_and_b32 v65, s68, v18\n"
      "v_lshlrev_b32_e64 v66, 16, s69\n"
      "v_and_b32 v67, s69, v18\n"
      "v_lshlrev_b32_e64 v68, 16, s72\n"
      "v_and_b32 v69, s72, v18\n"
      "v_lshlrev_b32_e64 v70, 16, s73\n"
      "v_and_b32 v71, s73, v18\n"
      "v_lshlrev_b32_e64 v72, 16, s8\n"
      "v_and_b32 v73, s8, v18\n"
      "v_lshlrev_b32_e64 v74, 16, s9\n"
      "v_and_b32 v75, s9, v18\n"
      "v_lshlrev_b32_e64 v76, 16, s12\n"
      "v_and_b32 v77, s12, v18\n"
      "v_lshlrev_b32_e64 v78, 16, s13\n"
      "v_and_b32 v79, s13, v18\n"
      "v_lshlrev_b32_e64 v80, 16, s96\n"
      "v_and_b32 v81, s96, v18\n"
      "v_lshlrev_b32_e64 v82, 16, s97\n"
      "v_and_b32 v83, s97, v18\n"
      "v_lshlrev_b32_e64 v84, 16, s4\n"
      "v_and_b32 v85, s4, v18\n"
      "v_lshlrev_b32_e64 v86, 16, s5\n"
      "v_and_b32 v87, s5, v18\n"
      "s_mov_b32 exec_lo, 0\n"
      "v_lshlrev_b32_e64 v24, 16, s54\n"
      "v_and_b32 v25, s54, v18\n"
      "v_lshlrev_b32_e64 v26, 16, s55\n"
      "v_and_b32 v27, s55, v18\n"
      "v_lshlrev_b32_e64 v28, 16, s58\n"
      "v_and_b32 v29, s58, v18\n"
      "v_lshlrev_b32_e64 v30, 16, s59\n"
      "v_and_b32 v31, s59, v18\n"
      "v_lshlrev_b32_e64 v32, 16, s62\n"
      "v_and_b32 v33, s62, v18\n"
      "v_lshlrev_b32_e64 v34, 16, s63\n"
      "v_and_b32 v35, s63, v18\n"
      "v_lshlrev_b32_e64 v36, 16, s66\n"
      "v_and_b32 v37, s66, v18\n"
      "v_lshlrev_b32_e64 v38, 16, s67\n"
      "v_and_b32 v39, s67, v18\n"
      "v_lshlrev_b32_e64 v40, 16, s82\n"
      "v_and_b32 v41, s82, v18\n"
      "v_lshlrev_b32_e64 v42, 16, s83\n"
      "v_and_b32 v43, s83, v18\n"
      "v_lshlrev_b32_e64 v44, 16, s86\n"
      "v_and_b32 v45, s86, v18\n"
      "v_lshlrev_b32_e64 v46, 16, s87\n"
      "v_and_b32 v47, s87, v18\n"
      "v_lshlrev_b32_e64 v48, 16, s90\n"
      "v_and_b32 v49, s90, v18\n"
      "v_lshlrev_b32_e64 v50, 16, s91\n"
      "v_and_b32 v51, s91, v18\n"
      "v_lshlrev_b32_e64 v52, 16, s94\n"
      "v_and_b32 v53, s94, v18\n"
      "v_lshlrev_b32_e64 v54, 16, s95\n"
      "v_and_b32 v55, s95, v18\n"
      "v_lshlrev_b32_e64 v56, 16, s26\n"
      "v_and_b32 v57, s26, v18\n"
      "v_lshlrev_b32_e64 v58, 16, s27\n"
      "v_and_b32 v59, s27, v18\n"
      "v_lshlrev_b32_e64 v60, 16, s30\n"
      "v_and_b32 v61, s30, v18\n"
      "v_lshlrev_b32_e64 v62, 16, s31\n"
      "v_and_b32 v63, s31, v18\n"
      "v_lshlrev_b32_e64 v64, 16, s70\n"
      "v_and_b32 v65, s70, v18\n"
      "v_lshlrev_b32_e64 v66, 16, s71\n"
      "v_and_b32 v67, s71, v18\n"
      "v_lshlrev_b32_e64 v68, 16, s74\n"
      "v_and_b32 v69, s74, v18\n"
      "v_lshlrev_b32_e64 v70, 16, s75\n"
      "v_and_b32 v71, s75, v18\n"
      "v_lshlrev_b32_e64 v72, 16, s10\n"
      "v_and_b32 v73, s10, v18\n"
      "v_lshlrev_b32_e64 v74, 16, s11\n"
      "v_and_b32 v75, s11, v18\n"
      "v_lshlrev_b32_e64 v76, 16, s14\n"
      "v_and_b32 v77, s14, v18\n"
      "v_lshlrev_b32_e64 v78, 16, s15\n"
      "v_and_b32 v79, s15, v18\n"
      "v_lshlrev_b32_e64 v80, 16, s98\n"
      "v_and_b32 v81, s98, v18\n"
      "v_lshlrev_b32_e64 v82, 16, s99\n"
      "v_and_b32 v83, s99, v18\n"
      "v_lshlrev_b32_e64 v84, 16, s6\n"
      "v_and_b32 v85, s6, v18\n"
      "v_lshlrev_b32_e64 v86, 16, s7\n"
      "v_and_b32 v87, s7, v18\n"
      "s_mov_b32 exec_lo, -1\n"
      "s_add_u32 s4, s20, 4096\n"
      "s_add_u32 s22, s21, 114688\n"
      "s_mul_i32 s51, s41, 16384\n"
      "s_add_u32 s24, s21, s51\n"
      "s_add_u32 s25, s24, 16384\n"
      "s_cmp_ge_u32 s25, s22\n"
      "s_cselect_b32 s51, 114688, 0\n"
      "s_sub_u32 s25, s25, s51\n"
      "s_add_u32 s26, s25, 16384\n"
      "s_cmp_ge_u32 s26, s22\n"
      "s_cselect_b32 s51, 114688, 0\n"
      "s_sub_u32 s26, s26, s51\n"
      "s_add_u32 s27, s26, 16384\n"
      "s_cmp_ge_u32 s27, s22\n"
      "s_cselect_b32 s51, 114688, 0\n"
      "s_sub_u32 s27, s27, s51\n"
      "s_add_u32 s28, s27, 16384\n"
      "s_cmp_ge_u32 s28, s22\n"
      "s_cselect_b32 s51, 114688, 0\n"
      "s_sub_u32 s28, s28, s51\n"
      "s_add_u32 s29, s28, 16384\n"
      "s_cmp_ge_u32 s29, s22\n"
      "s_cselect_b32 s51, 114688, 0\n"
      "s_sub_u32 s29, s29, s51\n"
      "s_add_u32 s30, s29, 16384\n"
      "s_cmp_ge_u32 s30, s22\n"
      "s_cselect_b32 s51, 114688, 0\n"
      "s_sub_u32 s30, s30, s51\n"
      "s_mov_b32 s52, 0xbdd2d3e8\n"
      "s_mov_b32 s53, 0xbdd2d3e8\n"
      "s_mov_b32 s54, 1.0\n"
      "s_mov_b32 s55, 1.0\n"
      "s_waitcnt lgkmcnt(0)\n"
      "s_barrier\n"
      "ds_read_b128 v[88:91], v4 offset:0\n"
      "ds_read_b64_tr_b16 v[204:205], v17 offset:0\n"
      "ds_read_b64_tr_b16 v[206:207], v17 offset:512\n"
      "ds_read_b64_tr_b16 v[200:201], v8 offset:0\n"
      "ds_read_b64_tr_b16 v[202:203], v8 offset:512\n"
      "ds_read_b128 v[92:95], v5 offset:0\n"
      "ds_read_b64_tr_b16 v[212:213], v17 offset:2048\n"
      "ds_read_b64_tr_b16 v[214:215], v17 offset:2560\n"
      "ds_read_b64_tr_b16 v[208:209], v8 offset:2048\n"
      "ds_read_b64_tr_b16 v[210:211], v8 offset:2560\n"
      "s_waitcnt lgkmcnt(7)\n"
      "v_mfma_f32_32x32x16_bf16 v[40:55], v[204:207], v[88:91], v[40:55]\n"
      "s_waitcnt lgkmcnt(5)\n"
      "v_mfma_f32_32x32x16_bf16 v[24:39], v[200:203], v[88:91], v[24:39]\n"
      "ds_read_b128 v[96:99], v6 offset:0\n"
      "ds_read_b64_tr_b16 v[220:221], v17 offset:4096\n"
      "ds_read_b64_tr_b16 v[222:223], v17 offset:4608\n"
      "s_waitcnt lgkmcnt(5)\n"
      "v_mfma_f32_32x32x16_bf16 v[40:55], v[212:215], v[92:95], v[40:55]\n"
      "ds_read_b64_tr_b16 v[216:217], v8 offset:4096\n"
      "ds_read_b64_tr_b16 v[218:219], v8 offset:4608\n"
      "ds_read_b128 v[100:103], v7 offset:0\n"
      "s_waitcnt lgkmcnt(6)\n"
      "v_mfma_f32_32x32x16_bf16 v[24:39], v[208:211], v[92:95], v[24:39]\n"
      "ds_read_b64_tr_b16 v[228:229], v17 offset:6144\n"
      "ds_read_b64_tr_b16 v[230:231], v17 offset:6656\n"
      "ds_read_b64_tr_b16 v[224:225], v8 offset:6144\n"
      "ds_read_b64_tr_b16 v[226:227], v8 offset:6656\n"
      "s_waitcnt lgkmcnt(0)\n"
      "s_barrier\n"
      "ds_read_b128 v[104:107], v4 offset:16384\n"
      "ds_read_b64_tr_b16 v[204:205], v17 offset:8192\n"
      "ds_read_b64_tr_b16 v[206:207], v17 offset:8704\n"
      "v_mfma_f32_32x32x16_bf16 v[40:55], v[220:223], v[96:99], v[40:55]\n"
      "ds_read_b64_tr_b16 v[200:201], v8 offset:8192\n"
      "ds_read_b64_tr_b16 v[202:203], v8 offset:8704\n"
      "ds_read_b128 v[108:111], v5 offset:16384\n"
      "v_mfma_f32_32x32x16_bf16 v[24:39], v[216:219], v[96:99], v[24:39]\n"
      "ds_read_b64_tr_b16 v[212:213], v17 offset:10240\n"
      "ds_read_b64_tr_b16 v[214:215], v17 offset:10752\n"
      "ds_read_b64_tr_b16 v[208:209], v8 offset:10240\n"
      "v_mfma_f32_32x32x16_bf16 v[40:55], v[228:231], v[100:103], v[40:55]\n"
      "ds_read_b64_tr_b16 v[210:211], v8 offset:10752\n"
      "ds_read_b128 v[112:115], v6 offset:16384\n"
      "ds_read_b64_tr_b16 v[220:221], v17 offset:12288\n"
      "v_mfma_f32_32x32x16_bf16 v[24:39], v[224:227], v[100:103], v[24:39]\n"
      "ds_read_b64_tr_b16 v[222:223], v17 offset:12800\n"
      "ds_read_b64_tr_b16 v[216:217], v8 offset:12288\n"
      "ds_read_b64_tr_b16 v[218:219], v8 offset:12800\n"
      "s_waitcnt lgkmcnt(12)\n"
      "v_mfma_f32_32x32x16_bf16 v[40:55], v[204:207], v[104:107], v[40:55]\n"
      "ds_read_b128 v[116:119], v7 offset:16384\n"
      "ds_read_b64_tr_b16 v[228:229], v17 offset:14336\n"
      "ds_read_b64_tr_b16 v[230:231], v17 offset:14848\n"
      "s_waitcnt lgkmcnt(13)\n"
      "v_mfma_f32_32x32x16_bf16 v[24:39], v[200:203], v[104:107], v[24:39]\n"
      "ds_read_b64_tr_b16 v[224:225], v8 offset:14336\n"
      "ds_read_b64_tr_b16 v[226:227], v8 offset:14848\n"
      "s_waitcnt lgkmcnt(12)\n"
      "v_mfma_f32_32x32x16_bf16 v[40:55], v[212:215], v[108:111], v[40:55]\n"
      "s_waitcnt lgkmcnt(10)\n"
      "v_mfma_f32_32x32x16_bf16 v[24:39], v[208:211], v[108:111], v[24:39]\n"
      "s_waitcnt lgkmcnt(0)\n"
      "s_barrier\n"
      "ds_read_b128 v[120:123], v4 offset:32768\n"
      "ds_read_b64_tr_b16 v[204:205], v17 offset:16384\n"
      "ds_read_b64_tr_b16 v[206:207], v17 offset:16896\n"
      "v_mfma_f32_32x32x16_bf16 v[40:55], v[220:223], v[112:115], v[40:55]\n"
      "ds_read_b64_tr_b16 v[200:201], v8 offset:16384\n"
      "ds_read_b64_tr_b16 v[202:203], v8 offset:16896\n"
      "ds_read_b128 v[124:127], v5 offset:32768\n"
      "v_mfma_f32_32x32x16_bf16 v[24:39], v[216:219], v[112:115], v[24:39]\n"
      "ds_read_b64_tr_b16 v[212:213], v17 offset:18432\n"
      "ds_read_b64_tr_b16 v[214:215], v17 offset:18944\n"
      "ds_read_b64_tr_b16 v[208:209], v8 offset:18432\n"
      "v_mfma_f32_32x32x16_bf16 v[40:55], v[228:231], v[116:119], v[40:55]\n"
      "ds_read_b64_tr_b16 v[210:211], v8 offset:18944\n"
      "ds_read_b128 v[128:131], v6 offset:32768\n"
      "ds_read_b64_tr_b16 v[220:221], v17 offset:20480\n"
      "v_mfma_f32_32x32x16_bf16 v[24:39], v[224:227], v[116:119], v[24:39]\n"
      "ds_read_b64_tr_b16 v[222:223], v17 offset:20992\n"
      "ds_read_b64_tr_b16 v[216:217], v8 offset:20480\n"
      "ds_read_b64_tr_b16 v[218:219], v8 offset:20992\n"
      "s_waitcnt lgkmcnt(12)\n"
      "v_mfma_f32_32x32x16_bf16 v[40:55], v[204:207], v[120:123], v[40:55]\n"
      "ds_read_b128 v[132:135], v7 offset:32768\n"
      "ds_read_b64_tr_b16 v[228:229], v17 offset:22528\n"
      "ds_read_b64_tr_b16 v[230:231], v17 offset:23040\n"
      "s_waitcnt lgkmcnt(13)\n"
      "v_mfma_f32_32x32x16_bf16 v[24:39], v[200:203], v[120:123], v[24:39]\n"
      "ds_read_b64_tr_b16 v[224:225], v8 offset:22528\n"
      "ds_read_b64_tr_b16 v[226:227], v8 offset:23040\n"
      "s_waitcnt lgkmcnt(12)\n"
      "v_mfma_f32_32x32x16_bf16 v[40:55], v[212:215], v[124:127], v[40:55]\n"
      "s_waitcnt lgkmcnt(10)\n"
      "v_mfma_f32_32x32x16_bf16 v[24:39], v[208:211], v[124:127], v[24:39]\n"
      "s_waitcnt lgkmcnt(0)\n"
      "s_barrier\n"
      "buffer_load_dwordx2 v[22:23], off, s[36:39], s23 sc1\n"
      "ds_read_b128 v[136:139], v4 offset:49152\n"
      "ds_read_b64_tr_b16 v[204:205], v17 offset:24576\n"
      "ds_read_b64_tr_b16 v[206:207], v17 offset:25088\n"
      "v_mfma_f32_32x32x16_bf16 v[40:55], v[220:223], v[128:131], v[40:55]\n"
      "ds_read_b64_tr_b16 v[200:201], v8 offset:24576\n"
      "ds_read_b64_tr_b16 v[202:203], v8 offset:25088\n"
      "ds_read_b128 v[140:143], v5 offset:49152\n"
      "v_mfma_f32_32x32x16_bf16 v[24:39], v[216:219], v[128:131], v[24:39]\n"
      "ds_read_b64_tr_b16 v[212:213], v17 offset:26624\n"
      "ds_read_b64_tr_b16 v[214:215], v17 offset:27136\n"
      "ds_read_b64_tr_b16 v[208:209], v8 offset:26624\n"
      "v_mfma_f32_32x32x16_bf16 v[40:55], v[228:231], v[132:135], v[40:55]\n"
      "ds_read_b64_tr_b16 v[210:211], v8 offset:27136\n"
      "ds_read_b128 v[144:147], v6 offset:49152\n"
      "ds_read_b64_tr_b16 v[220:221], v17 offset:28672\n"
      "v_mfma_f32_32x32x16_bf16 v[24:39], v[224:227], v[132:135], v[24:39]\n"
      "ds_read_b64_tr_b16 v[222:223], v17 offset:29184\n"
      "ds_read_b64_tr_b16 v[216:217], v8 offset:28672\n"
      "ds_read_b64_tr_b16 v[218:219], v8 offset:29184\n"
      "s_waitcnt lgkmcnt(12)\n"
      "v_mfma_f32_32x32x16_bf16 v[40:55], v[204:207], v[136:139], v[40:55]\n"
      "ds_read_b128 v[148:151], v7 offset:49152\n"
      "ds_read_b64_tr_b16 v[228:229], v17 offset:30720\n"
      "ds_read_b64_tr_b16 v[230:231], v17 offset:31232\n"
      "s_waitcnt lgkmcnt(13)\n"
      "v_mfma_f32_32x32x16_bf16 v[24:39], v[200:203], v[136:139], v[24:39]\n"
      "ds_read_b64_tr_b16 v[224:225], v8 offset:30720\n"
      "ds_read_b64_tr_b16 v[226:227], v8 offset:31232\n"
      "s_waitcnt lgkmcnt(12)\n"
      "v_mfma_f32_32x32x16_bf16 v[40:55], v[212:215], v[140:143], v[40:55]\n"
      "s_waitcnt lgkmcnt(10)\n"
      "v_mfma_f32_32x32x16_bf16 v[24:39], v[208:211], v[140:143], v[24:39]\n"
      "s_waitcnt lgkmcnt(0)\n"
      "s_barrier\n"
      "v_add_u32 v4, 65536, v4\n"
      "v_add_u32 v5, 65536, v5\n"
      "v_add_u32 v6, 65536, v6\n"
      "v_add_u32 v7, 65536, v7\n"
      "ds_read_b128 v[152:155], v4 offset:0\n"
      "ds_read_b64_tr_b16 v[204:205], v17 offset:32768\n"
      "ds_read_b64_tr_b16 v[206:207], v17 offset:33280\n"
      "v_mfma_f32_32x32x16_bf16 v[40:55], v[220:223], v[144:147], v[40:55]\n"
      "ds_read_b64_tr_b16 v[200:201], v8 offset:32768\n"
      "ds_read_b64_tr_b16 v[202:203], v8 offset:33280\n"
      "ds_read_b128 v[156:159], v5 offset:0\n"
      "v_mfma_f32_32x32x16_bf16 v[24:39], v[216:219], v[144:147], v[24:39]\n"
      "ds_read_b64_tr_b16 v[212:213], v17 offset:34816\n"
      "ds_read_b64_tr_b16 v[214:215], v17 offset:35328\n"
      "ds_read_b64_tr_b16 v[208:209], v8 offset:34816\n"
      "v_mfma_f32_32x32x16_bf16 v[40:55], v[228:231], v[148:151], v[40:55]\n"
      "ds_read_b64_tr_b16 v[210:211], v8 offset:35328\n"
      "ds_read_b128 v[160:163], v6 offset:0\n"
      "ds_read_b64_tr_b16 v[220:221], v17 offset:36864\n"
      "v_mfma_f32_32x32x16_bf16 v[24:39], v[224:227], v[148:151], v[24:39]\n"
      "ds_read_b64_tr_b16 v[222:223], v17 offset:37376\n"
      "ds_read_b64_tr_b16 v[216:217], v8 offset:36864\n"
      "ds_read_b64_tr_b16 v[218:219], v8 offset:37376\n"
      "s_waitcnt lgkmcnt(12)\n"
      "v_mfma_f32_32x32x16_bf16 v[40:55], v[204:207], v[152:155], v[40:55]\n"
      "ds_read_b128 v[164:167], v7 offset:0\n"
      "ds_read_b64_tr_b16 v[228:229], v17 offset:38912\n"
      "ds_read_b64_tr_b16 v[230:231], v17 offset:39424\n"
      "s_waitcnt lgkmcnt(13)\n"
      "v_mfma_f32_32x32x16_bf16 v[24:39], v[200:203], v[152:155], v[24:39]\n"
      "ds_read_b64_tr_b16 v[224:225], v8 offset:38912\n"
      "ds_read_b64_tr_b16 v[226:227], v8 offset:39424\n"
      "s_waitcnt lgkmcnt(12)\n"
      "v_mfma_f32_32x32x16_bf16 v[40:55], v[212:215], v[156:159], v[40:55]\n"
      "s_waitcnt lgkmcnt(10)\n"
      "v_mfma_f32_32x32x16_bf16 v[24:39], v[208:211], v[156:159], v[24:39]\n"
      "s_waitcnt lgkmcnt(0)\n"
      "s_barrier\n"
      "ds_read_b128 v[168:171], v4 offset:16384\n"
      "ds_read_b64_tr_b16 v[204:205], v17 offset:40960\n"
      "ds_read_b64_tr_b16 v[206:207], v17 offset:41472\n"
      "v_mfma_f32_32x32x16_bf16 v[40:55], v[220:223], v[160:163], v[40:55]\n"
      "ds_read_b64_tr_b16 v[200:201], v8 offset:40960\n"
      "ds_read_b64_tr_b16 v[202:203], v8 offset:41472\n"
      "ds_read_b128 v[172:175], v5 offset:16384\n"
      "v_mfma_f32_32x32x16_bf16 v[24:39], v[216:219], v[160:163], v[24:39]\n"
      "ds_read_b64_tr_b16 v[212:213], v17 offset:43008\n"
      "ds_read_b64_tr_b16 v[214:215], v17 offset:43520\n"
      "ds_read_b64_tr_b16 v[208:209], v8 offset:43008\n"
      "v_mfma_f32_32x32x16_bf16 v[40:55], v[228:231], v[164:167], v[40:55]\n"
      "ds_read_b64_tr_b16 v[210:211], v8 offset:43520\n"
      "ds_read_b128 v[176:179], v6 offset:16384\n"
      "ds_read_b64_tr_b16 v[220:221], v17 offset:45056\n"
      "v_mfma_f32_32x32x16_bf16 v[24:39], v[224:227], v[164:167], v[24:39]\n"
      "ds_read_b64_tr_b16 v[222:223], v17 offset:45568\n"
      "ds_read_b64_tr_b16 v[216:217], v8 offset:45056\n"
      "ds_read_b64_tr_b16 v[218:219], v8 offset:45568\n"
      "s_waitcnt lgkmcnt(12)\n"
      "v_mfma_f32_32x32x16_bf16 v[40:55], v[204:207], v[168:171], v[40:55]\n"
      "ds_read_b128 v[180:183], v7 offset:16384\n"
      "ds_read_b64_tr_b16 v[228:229], v17 offset:47104\n"
      "ds_read_b64_tr_b16 v[230:231], v17 offset:47616\n"
      "s_waitcnt lgkmcnt(13)\n"
      "v_mfma_f32_32x32x16_bf16 v[24:39], v[200:203], v[168:171], v[24:39]\n"
      "ds_read_b64_tr_b16 v[224:225], v8 offset:47104\n"
      "ds_read_b64_tr_b16 v[226:227], v8 offset:47616\n"
      "s_waitcnt lgkmcnt(12)\n"
      "v_mfma_f32_32x32x16_bf16 v[40:55], v[212:215], v[172:175], v[40:55]\n"
      "s_waitcnt lgkmcnt(10)\n"
      "v_mfma_f32_32x32x16_bf16 v[24:39], v[208:211], v[172:175], v[24:39]\n"
      "s_waitcnt lgkmcnt(0)\n"
      "s_barrier\n"
      "ds_read_b128 v[184:187], v4 offset:32768\n"
      "ds_read_b64_tr_b16 v[204:205], v17 offset:0\n"
      "ds_read_b64_tr_b16 v[206:207], v17 offset:512\n"
      "v_mfma_f32_32x32x16_bf16 v[40:55], v[220:223], v[176:179], v[40:55]\n"
      "ds_read_b64_tr_b16 v[200:201], v8 offset:0\n"
      "ds_read_b64_tr_b16 v[202:203], v8 offset:512\n"
      "ds_read_b128 v[188:191], v5 offset:32768\n"
      "v_mfma_f32_32x32x16_bf16 v[24:39], v[216:219], v[176:179], v[24:39]\n"
      "ds_read_b64_tr_b16 v[212:213], v17 offset:2048\n"
      "ds_read_b64_tr_b16 v[214:215], v17 offset:2560\n"
      "ds_read_b64_tr_b16 v[208:209], v8 offset:2048\n"
      "v_mfma_f32_32x32x16_bf16 v[40:55], v[228:231], v[180:183], v[40:55]\n"
      "ds_read_b64_tr_b16 v[210:211], v8 offset:2560\n"
      "ds_read_b128 v[192:195], v6 offset:32768\n"
      "ds_read_b64_tr_b16 v[220:221], v17 offset:4096\n"
      "v_mfma_f32_32x32x16_bf16 v[24:39], v[224:227], v[180:183], v[24:39]\n"
      "ds_read_b64_tr_b16 v[222:223], v17 offset:4608\n"
      "ds_read_b64_tr_b16 v[216:217], v8 offset:4096\n"
      "ds_read_b64_tr_b16 v[218:219], v8 offset:4608\n"
      "s_waitcnt lgkmcnt(12)\n"
      "v_mfma_f32_32x32x16_bf16 v[40:55], v[204:207], v[184:187], v[40:55]\n"
      "ds_read_b128 v[196:199], v7 offset:32768\n"
      "ds_read_b64_tr_b16 v[228:229], v17 offset:6144\n"
      "ds_read_b64_tr_b16 v[230:231], v17 offset:6656\n"
      "s_waitcnt lgkmcnt(13)\n"
      "v_mfma_f32_32x32x16_bf16 v[24:39], v[200:203], v[184:187], v[24:39]\n"
      "ds_read_b64_tr_b16 v[224:225], v8 offset:6144\n"
      "ds_read_b64_tr_b16 v[226:227], v8 offset:6656\n"
      "s_waitcnt lgkmcnt(12)\n"
      "v_mfma_f32_32x32x16_bf16 v[40:55], v[212:215], v[188:191], v[40:55]\n"
      "s_waitcnt lgkmcnt(10)\n"
      "v_mfma_f32_32x32x16_bf16 v[24:39], v[208:211], v[188:191], v[24:39]\n"
      "s_waitcnt lgkmcnt(0)\n"
      "s_barrier\n"
      "v_mfma_f32_32x32x16_bf16 v[24:39], v[216:219], v[192:195], v[24:39]\n"
      "ds_read_b64_tr_b16 v[200:201], v9 offset:0\n"
      "ds_read_b64_tr_b16 v[202:203], v9 offset:512\n"
      "ds_read_b64_tr_b16 v[204:205], v9 offset:2048\n"
      "v_mfma_f32_32x32x16_bf16 v[24:39], v[224:227], v[196:199], v[24:39]\n"
      "ds_read_b64_tr_b16 v[206:207], v9 offset:2560\n"
      "ds_read_b64_tr_b16 v[208:209], v9 offset:4096\n"
      "ds_read_b64_tr_b16 v[210:211], v9 offset:4608\n"
      "v_mfma_f32_32x32x16_bf16 v[40:55], v[220:223], v[192:195], v[40:55]\n"
      "ds_read_b64_tr_b16 v[212:213], v9 offset:6144\n"
      "ds_read_b64_tr_b16 v[214:215], v9 offset:6656\n"
      "v_mfma_f32_32x32x16_bf16 v[40:55], v[228:231], v[196:199], v[40:55]\n"
      "ds_read_b64_tr_b16 v[216:217], v9 offset:8192\n"
      "ds_read_b64_tr_b16 v[218:219], v9 offset:8704\n"
      "ds_read_b64_tr_b16 v[220:221], v9 offset:10240\n"
      "s_nop 1\n"
      "v_mul_f32 v232, v24, v24\n"
      "s_waitcnt lgkmcnt(3)\n"
      "v_mfma_f32_32x32x16_bf16 v[56:71], v[200:203], v[152:155], v[56:71]\n"
      "ds_read_b64_tr_b16 v[222:223], v9 offset:10752\n"
      "ds_read_b64_tr_b16 v[224:225], v9 offset:12288\n"
      "ds_read_b64_tr_b16 v[226:227], v9 offset:12800\n"
      "v_fmamk_f32 v232, v232, 0xbdd2d3e8, v20\n"
      "v_mul_f32 v233, v25, v25\n"
      "v_mul_f32 v232, v24, v232\n"
      "v_mfma_f32_32x32x16_bf16 v[56:71], v[204:207], v[156:159], v[56:71]\n"
      "ds_read_b64_tr_b16 v[228:229], v9 offset:14336\n"
      "ds_read_b64_tr_b16 v[230:231], v9 offset:14848\n"
      "v_fmamk_f32 v233, v233, 0xbdd2d3e8, v20\n"
      "v_exp_f32 v232, v232\n"
      "v_mul_f32 v234, v26, v26\n"
      "v_mfma_f32_32x32x16_bf16 v[56:71], v[208:211], v[160:163], v[56:71]\n"
      "ds_read_b64_tr_b16 v[200:201], v9 offset:16384\n"
      "ds_read_b64_tr_b16 v[202:203], v9 offset:16896\n"
      "v_mul_f32 v233, v25, v233\n"
      "v_add_f32 v232, 1.0, v232\n"
      "v_fmamk_f32 v234, v234, 0xbdd2d3e8, v20\n"
      "v_mul_f32 v235, v27, v27\n"
      "v_mfma_f32_32x32x16_bf16 v[56:71], v[212:215], v[164:167], v[56:71]\n"
      "ds_read_b64_tr_b16 v[204:205], v9 offset:18432\n"
      "ds_read_b64_tr_b16 v[206:207], v9 offset:18944\n"
      "v_exp_f32 v233, v233\n"
      "v_mul_f32 v234, v26, v234\n"
      "s_waitcnt lgkmcnt(4)\n"
      "v_mfma_f32_32x32x16_bf16 v[56:71], v[216:219], v[168:171], v[56:71]\n"
      "ds_read_b64_tr_b16 v[208:209], v9 offset:20480\n"
      "ds_read_b64_tr_b16 v[210:211], v9 offset:20992\n"
      "v_rcp_f32 v232, v232\n"
      "v_add_f32 v233, 1.0, v233\n"
      "v_fmamk_f32 v235, v235, 0xbdd2d3e8, v20\n"
      "v_mfma_f32_32x32x16_bf16 v[56:71], v[220:223], v[172:175], v[56:71]\n"
      "ds_read_b64_tr_b16 v[212:213], v9 offset:22528\n"
      "ds_read_b64_tr_b16 v[214:215], v9 offset:23040\n"
      "v_exp_f32 v234, v234\n"
      "v_mul_f32 v232, v24, v232\n"
      "v_mul_f32 v236, v28, v28\n"
      "v_mfma_f32_32x32x16_bf16 v[56:71], v[224:227], v[176:179], v[56:71]\n"
      "v_mul_f32 v235, v27, v235\n"
      "v_rcp_f32 v233, v233\n"
      "v_add_f32 v234, 1.0, v234\n"
      "v_fmamk_f32 v236, v236, 0xbdd2d3e8, v20\n"
      "v_mul_f32 v233, v25, v233\n"
      "v_mfma_f32_32x32x16_bf16 v[56:71], v[228:231], v[180:183], v[56:71]\n"
      "v_exp_f32 v235, v235\n"
      "v_mul_f32 v237, v29, v29\n"
      "v_mul_f32 v236, v28, v236\n"
      "s_waitcnt lgkmcnt(0)\n"
      "s_barrier\n"
      "v_mfma_f32_32x32x16_bf16 v[56:71], v[200:203], v[184:187], v[56:71]\n"
      "ds_read_b64_tr_b16 v[216:217], v9 offset:24576\n"
      "ds_read_b64_tr_b16 v[218:219], v9 offset:25088\n"
      "ds_read_b64_tr_b16 v[220:221], v9 offset:26624\n"
      "v_rcp_f32 v234, v234\n"
      "v_add_f32 v235, 1.0, v235\n"
      "v_mfma_f32_32x32x16_bf16 v[56:71], v[204:207], v[188:191], v[56:71]\n"
      "ds_read_b64_tr_b16 v[222:223], v9 offset:27136\n"
      "ds_read_b64_tr_b16 v[224:225], v9 offset:28672\n"
      "ds_read_b64_tr_b16 v[226:227], v9 offset:29184\n"
      "v_fmamk_f32 v237, v237, 0xbdd2d3e8, v20\n"
      "v_cvt_pk_bf16_f32 v240, v232, v233\n"
      "v_mul_f32 v234, v26, v234\n"
      "v_mfma_f32_32x32x16_bf16 v[56:71], v[208:211], v[192:195], v[56:71]\n"
      "ds_read_b64_tr_b16 v[228:229], v9 offset:30720\n"
      "ds_read_b64_tr_b16 v[230:231], v9 offset:31232\n"
      "ds_read_b64_tr_b16 v[200:201], v9 offset:32768\n"
      "v_exp_f32 v236, v236\n"
      "v_mul_f32 v238, v30, v30\n"
      "v_mfma_f32_32x32x16_bf16 v[56:71], v[212:215], v[196:199], v[56:71]\n"
      "ds_read_b64_tr_b16 v[202:203], v9 offset:33280\n"
      "ds_read_b64_tr_b16 v[204:205], v9 offset:34816\n"
      "ds_read_b64_tr_b16 v[206:207], v9 offset:35328\n"
      "v_mul_f32 v237, v29, v237\n"
      "v_add_f32 v236, 1.0, v236\n"
      "s_waitcnt lgkmcnt(4)\n"
      "v_mfma_f32_32x32x16_bf16 v[56:71], v[216:219], v[88:91], v[56:71]\n"
      "ds_read_b64_tr_b16 v[208:209], v9 offset:36864\n"
      "ds_read_b64_tr_b16 v[210:211], v9 offset:37376\n"
      "v_rcp_f32 v235, v235\n"
      "v_fmamk_f32 v238, v238, 0xbdd2d3e8, v20\n"
      "v_mul_f32 v235, v27, v235\n"
      "v_mfma_f32_32x32x16_bf16 v[56:71], v[220:223], v[92:95], v[56:71]\n"
      "ds_read_b64_tr_b16 v[212:213], v9 offset:38912\n"
      "ds_read_b64_tr_b16 v[214:215], v9 offset:39424\n"
      "v_exp_f32 v237, v237\n"
      "v_mul_f32 v239, v31, v31\n"
      "v_mul_f32 v238, v30, v238\n"
      "v_mfma_f32_32x32x16_bf16 v[56:71], v[224:227], v[96:99], v[56:71]\n"
      "ds_read_b64_tr_b16 v[216:217], v9 offset:40960\n"
      "ds_read_b64_tr_b16 v[218:219], v9 offset:41472\n"
      "v_rcp_f32 v236, v236\n"
      "v_add_f32 v237, 1.0, v237\n"
      "v_fmamk_f32 v239, v239, 0xbdd2d3e8, v20\n"
      "v_mfma_f32_32x32x16_bf16 v[56:71], v[228:231], v[100:103], v[56:71]\n"
      "ds_read_b64_tr_b16 v[220:221], v9 offset:43008\n"
      "ds_read_b64_tr_b16 v[222:223], v9 offset:43520\n"
      "v_cvt_pk_bf16_f32 v241, v234, v235\n"
      "v_exp_f32 v238, v238\n"
      "s_waitcnt lgkmcnt(4)\n"
      "v_mfma_f32_32x32x16_bf16 v[56:71], v[200:203], v[104:107], v[56:71]\n"
      "ds_read_b64_tr_b16 v[224:225], v9 offset:45056\n"
      "ds_read_b64_tr_b16 v[226:227], v9 offset:45568\n"
      "v_mul_f32 v236, v28, v236\n"
      "v_mul_f32 v232, v32, v32\n"
      "v_mul_f32 v239, v31, v239\n"
      "v_add_f32 v238, 1.0, v238\n"
      "v_mfma_f32_32x32x16_bf16 v[56:71], v[204:207], v[108:111], v[56:71]\n"
      "ds_read_b64_tr_b16 v[228:229], v9 offset:47104\n"
      "ds_read_b64_tr_b16 v[230:231], v9 offset:47616\n"
      "v_rcp_f32 v237, v237\n"
      "v_fmamk_f32 v232, v232, 0xbdd2d3e8, v20\n"
      "v_mul_f32 v237, v29, v237\n"
      "v_mfma_f32_32x32x16_bf16 v[56:71], v[208:211], v[112:115], v[56:71]\n"
      "ds_read_b64_tr_b16 v[200:201], v9 offset:49152\n"
      "ds_read_b64_tr_b16 v[202:203], v9 offset:49664\n"
      "v_exp_f32 v239, v239\n"
      "v_mul_f32 v233, v33, v33\n"
      "v_mul_f32 v232, v32, v232\n"
      "v_mfma_f32_32x32x16_bf16 v[56:71], v[212:215], v[116:119], v[56:71]\n"
      "ds_read_b64_tr_b16 v[204:205], v9 offset:51200\n"
      "ds_read_b64_tr_b16 v[206:207], v9 offset:51712\n"
      "v_rcp_f32 v238, v238\n"
      "v_add_f32 v239, 1.0, v239\n"
      "s_waitcnt lgkmcnt(4)\n"
      "v_mfma_f32_32x32x16_bf16 v[56:71], v[216:219], v[120:123], v[56:71]\n"
      "ds_read_b64_tr_b16 v[208:209], v9 offset:53248\n"
      "ds_read_b64_tr_b16 v[210:211], v9 offset:53760\n"
      "v_fmamk_f32 v233, v233, 0xbdd2d3e8, v20\n"
      "v_cvt_pk_bf16_f32 v242, v236, v237\n"
      "v_exp_f32 v232, v232\n"
      "v_mfma_f32_32x32x16_bf16 v[56:71], v[220:223], v[124:127], v[56:71]\n"
      "ds_read_b64_tr_b16 v[212:213], v9 offset:55296\n"
      "ds_read_b64_tr_b16 v[214:215], v9 offset:55808\n"
      "v_mul_f32 v238, v30, v238\n"
      "v_mul_f32 v234, v34, v34\n"
      "v_mul_f32 v233, v33, v233\n"
      "v_add_f32 v232, 1.0, v232\n"
      "v_mfma_f32_32x32x16_bf16 v[56:71], v[224:227], v[128:131], v[56:71]\n"
      "ds_read_b64_tr_b16 v[216:217], v19 offset:0\n"
      "ds_read_b64_tr_b16 v[218:219], v19 offset:512\n"
      "v_rcp_f32 v239, v239\n"
      "v_fmamk_f32 v234, v234, 0xbdd2d3e8, v20\n"
      "v_mul_f32 v239, v31, v239\n"
      "v_mfma_f32_32x32x16_bf16 v[56:71], v[228:231], v[132:135], v[56:71]\n"
      "ds_read_b64_tr_b16 v[220:221], v19 offset:2048\n"
      "ds_read_b64_tr_b16 v[222:223], v19 offset:2560\n"
      "v_exp_f32 v233, v233\n"
      "v_mul_f32 v235, v35, v35\n"
      "s_waitcnt lgkmcnt(4)\n"
      "v_mfma_f32_32x32x16_bf16 v[56:71], v[200:203], v[136:139], v[56:71]\n"
      "ds_read_b64_tr_b16 v[224:225], v19 offset:4096\n"
      "ds_read_b64_tr_b16 v[226:227], v19 offset:4608\n"
      "v_mul_f32 v234, v34, v234\n"
      "v_rcp_f32 v232, v232\n"
      "v_add_f32 v233, 1.0, v233\n"
      "v_mfma_f32_32x32x16_bf16 v[56:71], v[204:207], v[140:143], v[56:71]\n"
      "ds_read_b64_tr_b16 v[228:229], v19 offset:6144\n"
      "ds_read_b64_tr_b16 v[230:231], v19 offset:6656\n"
      "v_fmamk_f32 v235, v235, 0xbdd2d3e8, v20\n"
      "v_cvt_pk_bf16_f32 v243, v238, v239\n"
      "buffer_store_dwordx4 v[240:243], v16, s[36:39], s20 offen offset:0 sc1\n"
      "v_mul_f32 v232, v32, v232\n"
      "v_mfma_f32_32x32x16_bf16 v[56:71], v[208:211], v[144:147], v[56:71]\n"
      "ds_read_b64_tr_b16 v[200:201], v19 offset:8192\n"
      "ds_read_b64_tr_b16 v[202:203], v19 offset:8704\n"
      "v_exp_f32 v234, v234\n"
      "v_mul_f32 v236, v36, v36\n"
      "v_mul_f32 v235, v35, v235\n"
      "v_mfma_f32_32x32x16_bf16 v[56:71], v[212:215], v[148:151], v[56:71]\n"
      "ds_read_b64_tr_b16 v[204:205], v19 offset:10240\n"
      "ds_read_b64_tr_b16 v[206:207], v19 offset:10752\n"
      "v_rcp_f32 v233, v233\n"
      "v_add_f32 v234, 1.0, v234\n"
      "v_exp_f32 v235, v235\n"
      "v_mul_f32 v233, v33, v233\n"
      "v_rcp_f32 v234, v234\n"
      "v_add_f32 v235, 1.0, v235\n"
      "v_cvt_pk_bf16_f32 v244, v232, v233\n"
      "v_mul_f32 v234, v34, v234\n"
      "v_rcp_f32 v235, v235\n"
      "s_nop 0\n"
      "v_mul_f32 v235, v35, v235\n"
      "v_cvt_pk_bf16_f32 v245, v234, v235\n"
      "v_pk_mul_f32 v[232:233], v[36:37], v[36:37]\n"
      "v_pk_mul_f32 v[234:235], v[38:39], v[38:39]\n"
      "v_pk_fma_f32 v[232:233], v[232:233], s[52:53], v[20:21]\n"
      "v_pk_fma_f32 v[234:235], v[234:235], s[52:53], v[20:21]\n"
      "v_pk_mul_f32 v[232:233], v[36:37], v[232:233]\n"
      "v_pk_mul_f32 v[234:235], v[38:39], v[234:235]\n"
      "v_exp_f32 v232, v232\n"
      "v_exp_f32 v233, v233\n"
      "v_exp_f32 v234, v234\n"
      "v_exp_f32 v235, v235\n"
      "v_pk_add_f32 v[232:233], v[232:233], s[54:55]\n"
      "v_pk_add_f32 v[234:235], v[234:235], s[54:55]\n"
      "v_rcp_f32 v232, v232\n"
      "v_rcp_f32 v233, v233\n"
      "v_rcp_f32 v234, v234\n"
      "v_rcp_f32 v235, v235\n"
      "v_pk_mul_f32 v[232:233], v[36:37], v[232:233]\n"
      "v_pk_mul_f32 v[234:235], v[38:39], v[234:235]\n"
      "v_cvt_pk_bf16_f32 v246, v232, v233\n"
      "v_cvt_pk_bf16_f32 v247, v234, v235\n"
      "buffer_store_dwordx4 v[244:247], v16, s[36:39], s20 offen offset:1024 sc1\n"
      "v_pk_mul_f32 v[232:233], v[40:41], v[40:41]\n"
      "v_pk_mul_f32 v[234:235], v[42:43], v[42:43]\n"
      "v_pk_fma_f32 v[232:233], v[232:233], s[52:53], v[20:21]\n"
      "v_pk_fma_f32 v[234:235], v[234:235], s[52:53], v[20:21]\n"
      "v_pk_mul_f32 v[232:233], v[40:41], v[232:233]\n"
      "v_pk_mul_f32 v[234:235], v[42:43], v[234:235]\n"
      "v_exp_f32 v232, v232\n"
      "v_exp_f32 v233, v233\n"
      "v_exp_f32 v234, v234\n"
      "v_exp_f32 v235, v235\n"
      "v_pk_add_f32 v[232:233], v[232:233], s[54:55]\n"
      "v_pk_add_f32 v[234:235], v[234:235], s[54:55]\n"
      "v_rcp_f32 v232, v232\n"
      "v_rcp_f32 v233, v233\n"
      "v_rcp_f32 v234, v234\n"
      "v_rcp_f32 v235, v235\n"
      "v_pk_mul_f32 v[232:233], v[40:41], v[232:233]\n"
      "v_pk_mul_f32 v[234:235], v[42:43], v[234:235]\n"
      "v_cvt_pk_bf16_f32 v240, v232, v233\n"
      "v_cvt_pk_bf16_f32 v241, v234, v235\n"
      "v_pk_mul_f32 v[232:233], v[44:45], v[44:45]\n"
      "v_pk_mul_f32 v[234:235], v[46:47], v[46:47]\n"
      "v_pk_fma_f32 v[232:233], v[232:233], s[52:53], v[20:21]\n"
      "v_pk_fma_f32 v[234:235], v[234:235], s[52:53], v[20:21]\n"
      "v_pk_mul_f32 v[232:233], v[44:45], v[232:233]\n"
      "v_pk_mul_f32 v[234:235], v[46:47], v[234:235]\n"
      "v_exp_f32 v232, v232\n"
      "v_exp_f32 v233, v233\n"
      "v_exp_f32 v234, v234\n"
      "v_exp_f32 v235, v235\n"
      "v_pk_add_f32 v[232:233], v[232:233], s[54:55]\n"
      "v_pk_add_f32 v[234:235], v[234:235], s[54:55]\n"
      "v_rcp_f32 v232, v232\n"
      "v_rcp_f32 v233, v233\n"
      "v_rcp_f32 v234, v234\n"
      "v_rcp_f32 v235, v235\n"
      "v_pk_mul_f32 v[232:233], v[44:45], v[232:233]\n"
      "v_pk_mul_f32 v[234:235], v[46:47], v[234:235]\n"
      "v_cvt_pk_bf16_f32 v242, v232, v233\n"
      "v_cvt_pk_bf16_f32 v243, v234, v235\n"
      "buffer_store_dwordx4 v[240:243], v16, s[36:39], s20 offen offset:2048 sc1\n"
      "v_pk_mul_f32 v[232:233], v[48:49], v[48:49]\n"
      "v_pk_mul_f32 v[234:235], v[50:51], v[50:51]\n"
      "v_pk_fma_f32 v[232:233], v[232:233], s[52:53], v[20:21]\n"
      "v_pk_fma_f32 v[234:235], v[234:235], s[52:53], v[20:21]\n"
      "v_pk_mul_f32 v[232:233], v[48:49], v[232:233]\n"
      "v_pk_mul_f32 v[234:235], v[50:51], v[234:235]\n"
      "v_exp_f32 v232, v232\n"
      "v_exp_f32 v233, v233\n"
      "v_exp_f32 v234, v234\n"
      "v_exp_f32 v235, v235\n"
      "v_pk_add_f32 v[232:233], v[232:233], s[54:55]\n"
      "v_pk_add_f32 v[234:235], v[234:235], s[54:55]\n"
      "v_rcp_f32 v232, v232\n"
      "v_rcp_f32 v233, v233\n"
      "v_rcp_f32 v234, v234\n"
      "v_rcp_f32 v235, v235\n"
      "v_pk_mul_f32 v[232:233], v[48:49], v[232:233]\n"
      "v_pk_mul_f32 v[234:235], v[50:51], v[234:235]\n"
      "v_cvt_pk_bf16_f32 v244, v232, v233\n"
      "v_cvt_pk_bf16_f32 v245, v234, v235\n"
      "v_pk_mul_f32 v[232:233], v[52:53], v[52:53]\n"
      "v_pk_mul_f32 v[234:235], v[54:55], v[54:55]\n"
      "v_pk_fma_f32 v[232:233], v[232:233], s[52:53], v[20:21]\n"
      "v_pk_fma_f32 v[234:235], v[234:235], s[52:53], v[20:21]\n"
      "v_pk_mul_f32 v[232:233], v[52:53], v[232:233]\n"
      "v_pk_mul_f32 v[234:235], v[54:55], v[234:235]\n"
      "v_exp_f32 v232, v232\n"
      "v_exp_f32 v233, v233\n"
      "v_exp_f32 v234, v234\n"
      "v_exp_f32 v235, v235\n"
      "v_pk_add_f32 v[232:233], v[232:233], s[54:55]\n"
      "v_pk_add_f32 v[234:235], v[234:235], s[54:55]\n"
      "v_rcp_f32 v232, v232\n"
      "v_rcp_f32 v233, v233\n"
      "v_rcp_f32 v234, v234\n"
      "v_rcp_f32 v235, v235\n"
      "v_pk_mul_f32 v[232:233], v[52:53], v[232:233]\n"
      "v_pk_mul_f32 v[234:235], v[54:55], v[234:235]\n"
      "v_cvt_pk_bf16_f32 v246, v232, v233\n"
      "v_cvt_pk_bf16_f32 v247, v234, v235\n"
      "buffer_store_dwordx4 v[244:247], v16, s[36:39], s20 offen offset:3072 sc1\n"
      "s_nop 7\n"
      "v_pk_mul_f32 v[232:233], v[56:57], v[56:57]\n"
      "v_pk_mul_f32 v[234:235], v[58:59], v[58:59]\n"
      "v_pk_fma_f32 v[232:233], v[232:233], s[52:53], v[20:21]\n"
      "v_pk_fma_f32 v[234:235], v[234:235], s[52:53], v[20:21]\n"
      "v_pk_mul_f32 v[232:233], v[56:57], v[232:233]\n"
      "v_pk_mul_f32 v[234:235], v[58:59], v[234:235]\n"
      "v_exp_f32 v232, v232\n"
      "v_exp_f32 v233, v233\n"
      "v_exp_f32 v234, v234\n"
      "v_exp_f32 v235, v235\n"
      "v_pk_add_f32 v[232:233], v[232:233], s[54:55]\n"
      "v_pk_add_f32 v[234:235], v[234:235], s[54:55]\n"
      "v_rcp_f32 v232, v232\n"
      "v_rcp_f32 v233, v233\n"
      "v_rcp_f32 v234, v234\n"
      "v_rcp_f32 v235, v235\n"
      "v_pk_mul_f32 v[232:233], v[56:57], v[232:233]\n"
      "v_pk_mul_f32 v[234:235], v[58:59], v[234:235]\n"
      "v_cvt_pk_bf16_f32 v240, v232, v233\n"
      "v_cvt_pk_bf16_f32 v241, v234, v235\n"
      "s_waitcnt lgkmcnt(4)\n"
      "v_mfma_f32_32x32x16_bf16 v[72:87], v[216:219], v[152:155], v[72:87]\n"
      "ds_read_b64_tr_b16 v[208:209], v19 offset:12288\n"
      "ds_read_b64_tr_b16 v[210:211], v19 offset:12800\n"
      "v_mul_f32 v236, v60, v60\n"
      "v_fmamk_f32 v236, v236, 0xbdd2d3e8, v20\n"
      "v_mul_f32 v237, v61, v61\n"
      "v_mul_f32 v236, v60, v236\n"
      "v_mfma_f32_32x32x16_bf16 v[72:87], v[220:223], v[156:159], v[72:87]\n"
      "ds_read_b64_tr_b16 v[212:213], v19 offset:14336\n"
      "ds_read_b64_tr_b16 v[214:215], v19 offset:14848\n"
      "v_fmamk_f32 v237, v237, 0xbdd2d3e8, v20\n"
      "v_exp_f32 v236, v236\n"
      "v_mul_f32 v238, v62, v62\n"
      "v_mfma_f32_32x32x16_bf16 v[72:87], v[224:227], v[160:163], v[72:87]\n"
      "ds_read_b64_tr_b16 v[216:217], v19 offset:16384\n"
      "ds_read_b64_tr_b16 v[218:219], v19 offset:16896\n"
      "v_mul_f32 v237, v61, v237\n"
      "v_add_f32 v236, 1.0, v236\n"
      "v_fmamk_f32 v238, v238, 0xbdd2d3e8, v20\n"
      "v_mul_f32 v239, v63, v63\n"
      "v_mfma_f32_32x32x16_bf16 v[72:87], v[228:231], v[164:167], v[72:87]\n"
      "ds_read_b64_tr_b16 v[220:221], v19 offset:18432\n"
      "ds_read_b64_tr_b16 v[222:223], v19 offset:18944\n"
      "v_exp_f32 v237, v237\n"
      "v_mul_f32 v238, v62, v238\n"
      "s_waitcnt lgkmcnt(4)\n"
      "v_mfma_f32_32x32x16_bf16 v[72:87], v[200:203], v[168:171], v[72:87]\n"
      "ds_read_b64_tr_b16 v[224:225], v19 offset:20480\n"
      "ds_read_b64_tr_b16 v[226:227], v19 offset:20992\n"
      "v_rcp_f32 v236, v236\n"
      "v_add_f32 v237, 1.0, v237\n"
      "v_fmamk_f32 v239, v239, 0xbdd2d3e8, v20\n"
      "v_mfma_f32_32x32x16_bf16 v[72:87], v[204:207], v[172:175], v[72:87]\n"
      "ds_read_b64_tr_b16 v[228:229], v19 offset:22528\n"
      "ds_read_b64_tr_b16 v[230:231], v19 offset:23040\n"
      "v_exp_f32 v238, v238\n"
      "v_mul_f32 v236, v60, v236\n"
      "v_mul_f32 v232, v64, v64\n"
      "v_mfma_f32_32x32x16_bf16 v[72:87], v[208:211], v[176:179], v[72:87]\n"
      "ds_read_b64_tr_b16 v[200:201], v19 offset:24576\n"
      "ds_read_b64_tr_b16 v[202:203], v19 offset:25088\n"
      "v_mul_f32 v239, v63, v239\n"
      "v_rcp_f32 v237, v237\n"
      "v_add_f32 v238, 1.0, v238\n"
      "v_mfma_f32_32x32x16_bf16 v[72:87], v[212:215], v[180:183], v[72:87]\n"
      "ds_read_b64_tr_b16 v[204:205], v19 offset:26624\n"
      "ds_read_b64_tr_b16 v[206:207], v19 offset:27136\n"
      "v_fmamk_f32 v232, v232, 0xbdd2d3e8, v20\n"
      "v_exp_f32 v239, v239\n"
      "s_waitcnt lgkmcnt(4)\n"
      "v_mfma_f32_32x32x16_bf16 v[72:87], v[216:219], v[184:187], v[72:87]\n"
      "ds_read_b64_tr_b16 v[208:209], v19 offset:28672\n"
      "ds_read_b64_tr_b16 v[210:211], v19 offset:29184\n"
      "v_mul_f32 v237, v61, v237\n"
      "v_mul_f32 v233, v65, v65\n"
      "v_mul_f32 v232, v64, v232\n"
      "v_add_f32 v239, 1.0, v239\n"
      "v_mfma_f32_32x32x16_bf16 v[72:87], v[220:223], v[188:191], v[72:87]\n"
      "ds_read_b64_tr_b16 v[212:213], v19 offset:30720\n"
      "ds_read_b64_tr_b16 v[214:215], v19 offset:31232\n"
      "v_rcp_f32 v238, v238\n"
      "v_fmamk_f32 v233, v233, 0xbdd2d3e8, v20\n"
      "v_cvt_pk_bf16_f32 v242, v236, v237\n"
      "v_mfma_f32_32x32x16_bf16 v[72:87], v[224:227], v[192:195], v[72:87]\n"
      "v_exp_f32 v232, v232\n"
      "v_mul_f32 v238, v62, v238\n"
      "v_mul_f32 v234, v66, v66\n"
      "v_mul_f32 v233, v65, v233\n"
      "v_add_f32 v232, 1.0, v232\n"
      "v_mfma_f32_32x32x16_bf16 v[72:87], v[228:231], v[196:199], v[72:87]\n"
      "v_rcp_f32 v239, v239\n"
      "v_fmamk_f32 v234, v234, 0xbdd2d3e8, v20\n"
      "v_mul_f32 v239, v63, v239\n"
      "s_waitcnt lgkmcnt(0)\n"
      "s_barrier\n"
      "v_mfma_f32_32x32x16_bf16 v[72:87], v[200:203], v[88:91], v[72:87]\n"
      "ds_read_b64_tr_b16 v[216:217], v19 offset:32768\n"
      "ds_read_b64_tr_b16 v[218:219], v19 offset:33280\n"
      "ds_read_b64_tr_b16 v[220:221], v19 offset:34816\n"
      "v_exp_f32 v233, v233\n"
      "v_mul_f32 v235, v67, v67\n"
      "v_mfma_f32_32x32x16_bf16 v[72:87], v[204:207], v[92:95], v[72:87]\n"
      "ds_read_b64_tr_b16 v[222:223], v19 offset:35328\n"
      "ds_read_b64_tr_b16 v[224:225], v19 offset:36864\n"
      "ds_read_b64_tr_b16 v[226:227], v19 offset:37376\n"
      "v_mul_f32 v234, v66, v234\n"
      "v_rcp_f32 v232, v232\n"
      "v_mfma_f32_32x32x16_bf16 v[72:87], v[208:211], v[96:99], v[72:87]\n"
      "ds_read_b64_tr_b16 v[228:229], v19 offset:38912\n"
      "ds_read_b64_tr_b16 v[230:231], v19 offset:39424\n"
      "ds_read_b64_tr_b16 v[200:201], v19 offset:40960\n"
      "v_add_f32 v233, 1.0, v233\n"
      "v_fmamk_f32 v235, v235, 0xbdd2d3e8, v20\n"
      "v_cvt_pk_bf16_f32 v243, v238, v239\n"
      "v_mfma_f32_32x32x16_bf16 v[72:87], v[212:215], v[100:103], v[72:87]\n"
      "ds_read_b64_tr_b16 v[202:203], v19 offset:41472\n"
      "ds_read_b64_tr_b16 v[204:205], v19 offset:43008\n"
      "ds_read_b64_tr_b16 v[206:207], v19 offset:43520\n"
      "buffer_store_dwordx4 v[240:243], v16, s[36:39], s4 offen offset:0 sc1\n"
      "v_mul_f32 v232, v64, v232\n"
      "s_waitcnt lgkmcnt(4)\n"
      "v_mfma_f32_32x32x16_bf16 v[72:87], v[216:219], v[104:107], v[72:87]\n"
      "ds_read_b64_tr_b16 v[208:209], v19 offset:45056\n"
      "ds_read_b64_tr_b16 v[210:211], v19 offset:45568\n"
      "v_exp_f32 v234, v234\n"
      "v_mul_f32 v236, v68, v68\n"
      "v_mul_f32 v235, v67, v235\n"
      "v_mfma_f32_32x32x16_bf16 v[72:87], v[220:223], v[108:111], v[72:87]\n"
      "ds_read_b64_tr_b16 v[212:213], v19 offset:47104\n"
      "ds_read_b64_tr_b16 v[214:215], v19 offset:47616\n"
      "v_rcp_f32 v233, v233\n"
      "v_add_f32 v234, 1.0, v234\n"
      "v_fmamk_f32 v236, v236, 0xbdd2d3e8, v20\n"
      "v_mfma_f32_32x32x16_bf16 v[72:87], v[224:227], v[112:115], v[72:87]\n"
      "ds_read_b64_tr_b16 v[216:217], v19 offset:49152\n"
      "ds_read_b64_tr_b16 v[218:219], v19 offset:49664\n"
      "v_exp_f32 v235, v235\n"
      "v_mul_f32 v233, v65, v233\n"
      "v_mul_f32 v237, v69, v69\n"
      "v_mfma_f32_32x32x16_bf16 v[72:87], v[228:231], v[116:119], v[72:87]\n"
      "ds_read_b64_tr_b16 v[220:221], v19 offset:51200\n"
      "ds_read_b64_tr_b16 v[222:223], v19 offset:51712\n"
      "v_mul_f32 v236, v68, v236\n"
      "v_rcp_f32 v234, v234\n"
      "s_waitcnt lgkmcnt(4)\n"
      "v_mfma_f32_32x32x16_bf16 v[72:87], v[200:203], v[120:123], v[72:87]\n"
      "ds_read_b64_tr_b16 v[224:225], v19 offset:53248\n"
      "ds_read_b64_tr_b16 v[226:227], v19 offset:53760\n"
      "v_add_f32 v235, 1.0, v235\n"
      "v_fmamk_f32 v237, v237, 0xbdd2d3e8, v20\n"
      "v_cvt_pk_bf16_f32 v244, v232, v233\n"
      "v_mul_f32 v234, v66, v234\n"
      "v_mfma_f32_32x32x16_bf16 v[72:87], v[204:207], v[124:127], v[72:87]\n"
      "ds_read_b64_tr_b16 v[228:229], v19 offset:55296\n"
      "ds_read_b64_tr_b16 v[230:231], v19 offset:55808\n"
      "v_exp_f32 v236, v236\n"
      "v_mul_f32 v238, v70, v70\n"
      "v_mul_f32 v237, v69, v237\n"
      "v_mfma_f32_32x32x16_bf16 v[72:87], v[208:211], v[128:131], v[72:87]\n"
      "v_rcp_f32 v235, v235\n"
      "v_add_f32 v236, 1.0, v236\n"
      "v_fmamk_f32 v238, v238, 0xbdd2d3e8, v20\n"
      "v_exp_f32 v237, v237\n"
      "v_mfma_f32_32x32x16_bf16 v[72:87], v[212:215], v[132:135], v[72:87]\n"
      "v_mul_f32 v235, v67, v235\n"
      "v_mul_f32 v239, v71, v71\n"
      "v_mul_f32 v238, v70, v238\n"
      "v_rcp_f32 v236, v236\n"
      "s_waitcnt lgkmcnt(0)\n"
      "v_mfma_f32_32x32x16_bf16 v[72:87], v[216:219], v[136:139], v[72:87]\n"
      "v_add_f32 v237, 1.0, v237\n"
      "v_fmamk_f32 v239, v239, 0xbdd2d3e8, v20\n"
      "v_cvt_pk_bf16_f32 v245, v234, v235\n"
      "v_exp_f32 v238, v238\n"
      "v_mul_f32 v236, v68, v236\n"
      "v_mfma_f32_32x32x16_bf16 v[72:87], v[220:223], v[140:143], v[72:87]\n"
      "v_mul_f32 v239, v71, v239\n"
      "v_rcp_f32 v237, v237\n"
      "v_add_f32 v238, 1.0, v238\n"
      "v_exp_f32 v239, v239\n"
      "v_mfma_f32_32x32x16_bf16 v[72:87], v[224:227], v[144:147], v[72:87]\n"
      "v_mul_f32 v237, v69, v237\n"
      "v_rcp_f32 v238, v238\n"
      "v_add_f32 v239, 1.0, v239\n"
      "v_cvt_pk_bf16_f32 v246, v236, v237\n"
      "v_mul_f32 v238, v70, v238\n"
      "v_mfma_f32_32x32x16_bf16 v[72:87], v[228:231], v[148:151], v[72:87]\n"
      "v_rcp_f32 v239, v239\n"
      "s_nop 0\n"
      "v_mul_f32 v239, v71, v239\n"
      "s_waitcnt vmcnt(5)\n"
      "s_waitcnt lgkmcnt(0)\n"
      "s_barrier\n"
      "v_cvt_pk_bf16_f32 v247, v238, v239\n"
      "buffer_store_dwordx4 v[244:247], v16, s[36:39], s4 offen offset:1024 sc1\n"
      "v_add_u32 v22, 0x9E3779B9, v22\n"
      "v_add_u32 v23, 0x7F4A7C15, v23\n"
      "s_nop 7\n"
      "v_pk_mul_f32 v[232:233], v[72:73], v[72:73]\n"
      "v_pk_mul_f32 v[234:235], v[74:75], v[74:75]\n"
      "v_pk_fma_f32 v[232:233], v[232:233], s[52:53], v[20:21]\n"
      "v_pk_fma_f32 v[234:235], v[234:235], s[52:53], v[20:21]\n"
      "v_pk_mul_f32 v[232:233], v[72:73], v[232:233]\n"
      "v_pk_mul_f32 v[234:235], v[74:75], v[234:235]\n"
      "v_exp_f32 v232, v232\n"
      "v_exp_f32 v233, v233\n"
      "v_exp_f32 v234, v234\n"
      "v_exp_f32 v235, v235\n"
      "v_pk_add_f32 v[232:233], v[232:233], s[54:55]\n"
      "v_pk_add_f32 v[234:235], v[234:235], s[54:55]\n"
      "v_rcp_f32 v232, v232\n"
      "v_rcp_f32 v233, v233\n"
      "v_rcp_f32 v234, v234\n"
      "v_rcp_f32 v235, v235\n"
      "v_pk_mul_f32 v[232:233], v[72:73], v[232:233]\n"
      "v_pk_mul_f32 v[234:235], v[74:75], v[234:235]\n"
      "v_cvt_pk_bf16_f32 v240, v232, v233\n"
      "v_cvt_pk_bf16_f32 v241, v234, v235\n"
      "v_pk_mul_f32 v[232:233], v[76:77], v[76:77]\n"
      "v_pk_mul_f32 v[234:235], v[78:79], v[78:79]\n"
      "v_pk_fma_f32 v[232:233], v[232:233], s[52:53], v[20:21]\n"
      "v_pk_fma_f32 v[234:235], v[234:235], s[52:53], v[20:21]\n"
      "v_pk_mul_f32 v[232:233], v[76:77], v[232:233]\n"
      "v_pk_mul_f32 v[234:235], v[78:79], v[234:235]\n"
      "v_exp_f32 v232, v232\n"
      "v_exp_f32 v233, v233\n"
      "v_exp_f32 v234, v234\n"
      "v_exp_f32 v235, v235\n"
      "v_pk_add_f32 v[232:233], v[232:233], s[54:55]\n"
      "v_pk_add_f32 v[234:235], v[234:235], s[54:55]\n"
      "v_rcp_f32 v232, v232\n"
      "v_rcp_f32 v233, v233\n"
      "v_rcp_f32 v234, v234\n"
      "v_rcp_f32 v235, v235\n"
      "v_pk_mul_f32 v[232:233], v[76:77], v[232:233]\n"
      "v_pk_mul_f32 v[234:235], v[78:79], v[234:235]\n"
      "v_cvt_pk_bf16_f32 v242, v232, v233\n"
      "v_cvt_pk_bf16_f32 v243, v234, v235\n"
      "buffer_store_dwordx4 v[240:243], v16, s[36:39], s4 offen offset:2048 sc1\n"
      "v_pk_mul_f32 v[232:233], v[80:81], v[80:81]\n"
      "v_pk_mul_f32 v[234:235], v[82:83], v[82:83]\n"
      "v_pk_fma_f32 v[232:233], v[232:233], s[52:53], v[20:21]\n"
      "v_pk_fma_f32 v[234:235], v[234:235], s[52:53], v[20:21]\n"
      "v_pk_mul_f32 v[232:233], v[80:81], v[232:233]\n"
      "v_pk_mul_f32 v[234:235], v[82:83], v[234:235]\n"
      "v_exp_f32 v232, v232\n"
      "v_exp_f32 v233, v233\n"
      "v_exp_f32 v234, v234\n"
      "v_exp_f32 v235, v235\n"
      "v_pk_add_f32 v[232:233], v[232:233], s[54:55]\n"
      "v_pk_add_f32 v[234:235], v[234:235], s[54:55]\n"
      "v_rcp_f32 v232, v232\n"
      "v_rcp_f32 v233, v233\n"
      "v_rcp_f32 v234, v234\n"
      "v_rcp_f32 v235, v235\n"
      "v_pk_mul_f32 v[232:233], v[80:81], v[232:233]\n"
      "v_pk_mul_f32 v[234:235], v[82:83], v[234:235]\n"
      "v_cvt_pk_bf16_f32 v244, v232, v233\n"
      "v_cvt_pk_bf16_f32 v245, v234, v235\n"
      "v_pk_mul_f32 v[232:233], v[84:85], v[84:85]\n"
      "v_pk_mul_f32 v[234:235], v[86:87], v[86:87]\n"
      "v_pk_fma_f32 v[232:233], v[232:233], s[52:53], v[20:21]\n"
      "v_pk_fma_f32 v[234:235], v[234:235], s[52:53], v[20:21]\n"
      "v_pk_mul_f32 v[232:233], v[84:85], v[232:233]\n"
      "v_pk_mul_f32 v[234:235], v[86:87], v[234:235]\n"
      "v_exp_f32 v232, v232\n"
      "v_exp_f32 v233, v233\n"
      "v_exp_f32 v234, v234\n"
      "v_exp_f32 v235, v235\n"
      "v_pk_add_f32 v[232:233], v[232:233], s[54:55]\n"
      "v_pk_add_f32 v[234:235], v[234:235], s[54:55]\n"
      "v_rcp_f32 v232, v232\n"
      "v_rcp_f32 v233, v233\n"
      "v_rcp_f32 v234, v234\n"
      "v_rcp_f32 v235, v235\n"
      "v_pk_mul_f32 v[232:233], v[84:85], v[232:233]\n"
      "v_pk_mul_f32 v[234:235], v[86:87], v[234:235]\n"
      "v_cvt_pk_bf16_f32 v246, v232, v233\n"
      "v_cvt_pk_bf16_f32 v247, v234, v235\n"
      "buffer_store_dwordx4 v[244:247], v16, s[36:39], s4 offen offset:3072 sc1\n"
      "s_waitcnt lgkmcnt(0)\n"
      "s_barrier\n"
      "ds_read_b64_tr_b16 v[56:57], v14 offset:0\n"
      "ds_read_b64_tr_b16 v[58:59], v14 offset:1024\n"
      "ds_read_b64_tr_b16 v[60:61], v15 offset:0\n"
      "ds_read_b64_tr_b16 v[62:63], v15 offset:1024\n"
      "ds_read_b64_tr_b16 v[64:65], v14 offset:2048\n"
      "ds_read_b64_tr_b16 v[66:67], v14 offset:3072\n"
      "ds_read_b64_tr_b16 v[68:69], v15 offset:2048\n"
      "ds_read_b64_tr_b16 v[70:71], v15 offset:3072\n"
      "ds_read_b64_tr_b16 v[72:73], v14 offset:4096\n"
      "ds_read_b64_tr_b16 v[74:75], v14 offset:5120\n"
      "ds_read_b64_tr_b16 v[76:77], v15 offset:4096\n"
      "ds_read_b64_tr_b16 v[78:79], v15 offset:5120\n"
      "ds_read_b64_tr_b16 v[80:81], v14 offset:6144\n"
      "ds_read_b64_tr_b16 v[82:83], v14 offset:7168\n"
      "ds_read_b64_tr_b16 v[84:85], v15 offset:6144\n"
      "s_waitcnt lgkmcnt(7)\n"
      "ds_read_b64_tr_b16 v[86:87], v15 offset:7168\n"
      "ds_read_b64_tr_b16 v[88:89], v12 offset:0\n"
      "ds_read_b64_tr_b16 v[90:91], v12 offset:1024\n"
      "ds_read_b64_tr_b16 v[92:93], v13 offset:0\n"
      "ds_read_b64_tr_b16 v[94:95], v13 offset:1024\n"
      "ds_read_b64_tr_b16 v[96:97], v12 offset:2048\n"
      "ds_read_b64_tr_b16 v[98:99], v12 offset:3072\n"
      "ds_read_b64_tr_b16 v[100:101], v13 offset:2048\n"
      "s_waitcnt lgkmcnt(7)\n"
      "ds_read_b64_tr_b16 v[102:103], v13 offset:3072\n"
      "ds_read_b64_tr_b16 v[104:105], v12 offset:4096\n"
      "ds_read_b64_tr_b16 v[106:107], v12 offset:5120\n"
      "ds_read_b64_tr_b16 v[108:109], v13 offset:4096\n"
      "ds_read_b64_tr_b16 v[110:111], v13 offset:5120\n"
      "ds_read_b64_tr_b16 v[112:113], v12 offset:6144\n"
      "ds_read_b64_tr_b16 v[114:115], v12 offset:7168\n"
      "ds_read_b64_tr_b16 v[116:117], v13 offset:6144\n"
      "s_waitcnt lgkmcnt(7)\n"
      "ds_read_b64_tr_b16 v[118:119], v13 offset:7168\n"
      "ds_read_b64_tr_b16 v[120:121], v12 offset:16384\n"
      "ds_read_b64_tr_b16 v[122:123], v12 offset:17408\n"
      "ds_read_b64_tr_b16 v[124:125], v13 offset:16384\n"
      "ds_read_b64_tr_b16 v[126:127], v13 offset:17408\n"
      "ds_read_b64_tr_b16 v[128:129], v12 offset:18432\n"
      "ds_read_b64_tr_b16 v[130:131], v12 offset:19456\n"
      "ds_read_b64_tr_b16 v[132:133], v13 offset:18432\n"
      "s_waitcnt lgkmcnt(7)\n"
      "ds_read_b64_tr_b16 v[134:135], v13 offset:19456\n"
      "ds_read_b64_tr_b16 v[136:137], v12 offset:20480\n"
      "ds_read_b64_tr_b16 v[138:139], v12 offset:21504\n"
      "ds_read_b64_tr_b16 v[140:141], v13 offset:20480\n"
      "ds_read_b64_tr_b16 v[142:143], v13 offset:21504\n"
      "ds_read_b64_tr_b16 v[144:145], v12 offset:22528\n"
      "ds_read_b64_tr_b16 v[146:147], v12 offset:23552\n"
      "ds_read_b64_tr_b16 v[148:149], v13 offset:22528\n"
      "s_waitcnt lgkmcnt(7)\n"
      "ds_read_b64_tr_b16 v[150:151], v13 offset:23552\n"
      "ds_read_b64_tr_b16 v[152:153], v14 offset:16384\n"
      "ds_read_b64_tr_b16 v[154:155], v14 offset:17408\n"
      "ds_read_b64_tr_b16 v[156:157], v15 offset:16384\n"
      "ds_read_b64_tr_b16 v[158:159], v15 offset:17408\n"
      "ds_read_b64_tr_b16 v[160:161], v14 offset:18432\n"
      "ds_read_b64_tr_b16 v[162:163], v14 offset:19456\n"
      "ds_read_b64_tr_b16 v[164:165], v15 offset:18432\n"
      "s_waitcnt lgkmcnt(7)\n"
      "ds_read_b64_tr_b16 v[166:167], v15 offset:19456\n"
      "ds_read_b64_tr_b16 v[168:169], v14 offset:20480\n"
      "ds_read_b64_tr_b16 v[170:171], v14 offset:21504\n"
      "ds_read_b64_tr_b16 v[172:173], v15 offset:20480\n"
      "ds_read_b64_tr_b16 v[174:175], v15 offset:21504\n"
      "ds_read_b64_tr_b16 v[176:177], v14 offset:22528\n"
      "ds_read_b64_tr_b16 v[178:179], v14 offset:23552\n"
      "ds_read_b64_tr_b16 v[180:181], v15 offset:22528\n"
      "s_waitcnt lgkmcnt(7)\n"
      "ds_read_b64_tr_b16 v[182:183], v15 offset:23552\n"
      "v_lshlrev_b32 v24, 16, v248\n"
      "v_and_b32 v25, v248, v18\n"
      "v_lshlrev_b32 v26, 16, v249\n"
      "v_and_b32 v27, v249, v18\n"
      "v_lshlrev_b32 v28, 16, v250\n"
      "v_and_b32 v29, v250, v18\n"
      "v_lshlrev_b32 v30, 16, v251\n"
      "v_and_b32 v31, v251, v18\n"
      "v_lshlrev_b32 v32, 16, v252\n"
      "v_and_b32 v33, v252, v18\n"
      "v_lshlrev_b32 v34, 16, v253\n"
      "v_and_b32 v35, v253, v18\n"
      "v_lshlrev_b32 v36, 16, v254\n"
      "v_and_b32 v37, v254, v18\n"
      "v_lshlrev_b32 v38, 16, v255\n"
      "v_and_b32 v39, v255, v18\n"
      "s_lshl_b32 s49, s47, 12\n"
      "s_add_u32 s49, s49, 98304\n"
      "v_add_u32 v4, s49, v16\n"
      "v_xor_b32 v5, 4096, v4\n"
      "v_xor_b32 v6, 8192, v4\n"
      "v_xor_b32 v7, 12288, v4\n"
      "s_waitcnt vmcnt(0)\n"
      "s_mov_b64 exec, 1\n"
      "buffer_store_dwordx2 v[22:23], off, s[36:39], s50 sc1\n"
      "s_mov_b64 exec, -1\n"
      "s_waitcnt lgkmcnt(0)\n"
      "s_barrier\n"
      "s_setprio 2\n"
      "s_barrier\n"
      "buffer_load_dwordx4 v[184:187], v16, s[36:39], s24 offen offset:0 sc1\n"
      "buffer_load_dwordx4 v[188:191], v16, s[36:39], s24 offen offset:1024 sc1\n"
      "buffer_load_dwordx4 v[192:195], v16, s[36:39], s24 offen offset:2048 sc1\n"
      "buffer_load_dwordx4 v[196:199], v16, s[36:39], s24 offen offset:3072 sc1\n"
      "buffer_load_dwordx4 v[200:203], v16, s[36:39], s25 offen offset:0 sc1\n"
      "buffer_load_dwordx4 v[204:207], v16, s[36:39], s25 offen offset:1024 sc1\n"
      "buffer_load_dwordx4 v[208:211], v16, s[36:39], s25 offen offset:2048 sc1\n"
      "buffer_load_dwordx4 v[212:215], v16, s[36:39], s25 offen offset:3072 sc1\n"
      "s_waitcnt vmcnt(7)\n"
      "v_mfma_f32_32x32x16_bf16 v[40:55], v[60:63], v[184:187], 0\n"
      "buffer_load_dwordx4 v[216:219], v16, s[36:39], s26 offen offset:0 sc1\n"
      "v_mfma_f32_32x32x16_bf16 v[24:39], v[56:59], v[184:187], v[24:39]\n"
      "buffer_load_dwordx4 v[220:223], v16, s[36:39], s26 offen offset:1024 sc1\n"
      "s_waitcnt vmcnt(8)\n"
      "v_mfma_f32_32x32x16_bf16 v[40:55], v[68:71], v[188:191], v[40:55]\n"
      "buffer_load_dwordx4 v[224:227], v16, s[36:39], s26 offen offset:2048 sc1\n"
      "v_mfma_f32_32x32x16_bf16 v[24:39], v[64:67], v[188:191], v[24:39]\n"
      "buffer_load_dwordx4 v[228:231], v16, s[36:39], s26 offen offset:3072 sc1\n"
      "s_waitcnt vmcnt(9)\n"
      "v_mfma_f32_32x32x16_bf16 v[40:55], v[76:79], v[192:195], v[40:55]\n"
      "buffer_load_dwordx4 v[232:235], v16, s[36:39], s27 offen offset:0 sc1\n"
      "v_mfma_f32_32x32x16_bf16 v[24:39], v[72:75], v[192:195], v[24:39]\n"
      "buffer_load_dwordx4 v[236:239], v16, s[36:39], s27 offen offset:1024 sc1\n"
      "s_waitcnt vmcnt(10)\n"
      "v_mfma_f32_32x32x16_bf16 v[40:55], v[84:87], v[196:199], v[40:55]\n"
      "buffer_load_dwordx4 v[240:243], v16, s[36:39], s27 offen offset:2048 sc1\n"
      "v_mfma_f32_32x32x16_bf16 v[24:39], v[80:83], v[196:199], v[24:39]\n"
      "buffer_load_dwordx4 v[244:247], v16, s[36:39], s27 offen offset:3072 sc1\n"
      "s_waitcnt vmcnt(11)\n"
      "v_mfma_f32_32x32x16_bf16 v[40:55], v[92:95], v[200:203], v[40:55]\n"
      "buffer_load_dwordx4 v[184:187], v16, s[36:39], s28 offen offset:0 sc1\n"
      "ds_read_b64_tr_b16 v[56:57], v12 offset:32768\n"
      "ds_read_b64_tr_b16 v[58:59], v12 offset:33792\n"
      "v_mfma_f32_32x32x16_bf16 v[24:39], v[88:91], v[200:203], v[24:39]\n"
      "ds_read_b64_tr_b16 v[60:61], v13 offset:32768\n"
      "ds_read_b64_tr_b16 v[62:63], v13 offset:33792\n"
      "buffer_load_dwordx4 v[188:191], v16, s[36:39], s28 offen offset:1024 sc1\n"
      "s_waitcnt vmcnt(12)\n"
      "v_mfma_f32_32x32x16_bf16 v[40:55], v[100:103], v[204:207], v[40:55]\n"
      "ds_read_b64_tr_b16 v[64:65], v12 offset:34816\n"
      "ds_read_b64_tr_b16 v[66:67], v12 offset:35840\n"
      "ds_read_b64_tr_b16 v[68:69], v13 offset:34816\n"
      "v_mfma_f32_32x32x16_bf16 v[24:39], v[96:99], v[204:207], v[24:39]\n"
      "ds_read_b64_tr_b16 v[70:71], v13 offset:35840\n"
      "buffer_load_dwordx4 v[192:195], v16, s[36:39], s28 offen offset:2048 sc1\n"
      "ds_read_b64_tr_b16 v[72:73], v12 offset:36864\n"
      "s_waitcnt vmcnt(12)\n"
      "v_mfma_f32_32x32x16_bf16 v[40:55], v[108:111], v[208:211], v[40:55]\n"
      "ds_read_b64_tr_b16 v[74:75], v12 offset:37888\n"
      "ds_read_b64_tr_b16 v[76:77], v13 offset:36864\n"
      "ds_read_b64_tr_b16 v[78:79], v13 offset:37888\n"
      "v_mfma_f32_32x32x16_bf16 v[24:39], v[104:107], v[208:211], v[24:39]\n"
      "buffer_load_dwordx4 v[196:199], v16, s[36:39], s28 offen offset:3072 sc1\n"
      "ds_read_b64_tr_b16 v[80:81], v12 offset:38912\n"
      "ds_read_b64_tr_b16 v[82:83], v12 offset:39936\n"
      "s_waitcnt vmcnt(12)\n"
      "v_mfma_f32_32x32x16_bf16 v[40:55], v[116:119], v[212:215], v[40:55]\n"
      "ds_read_b64_tr_b16 v[84:85], v13 offset:38912\n"
      "s_waitcnt lgkmcnt(7)\n"
      "ds_read_b64_tr_b16 v[86:87], v13 offset:39936\n"
      "v_mfma_f32_32x32x16_bf16 v[24:39], v[112:115], v[212:215], v[24:39]\n"
      "s_waitcnt lgkmcnt(0)\n"
      "s_barrier\n"
      "s_waitcnt vmcnt(11)\n"
      "v_mfma_f32_32x32x16_bf16 v[40:55], v[124:127], v[216:219], v[40:55]\n"
      "buffer_load_dwordx4 v[200:203], v16, s[36:39], s29 offen offset:0 sc1\n"
      "ds_read_b64_tr_b16 v[88:89], v12 offset:49152\n"
      "ds_read_b64_tr_b16 v[90:91], v12 offset:50176\n"
      "v_mfma_f32_32x32x16_bf16 v[24:39], v[120:123], v[216:219], v[24:39]\n"
      "ds_read_b64_tr_b16 v[92:93], v13 offset:49152\n"
      "ds_read_b64_tr_b16 v[94:95], v13 offset:50176\n"
      "buffer_load_dwordx4 v[204:207], v16, s[36:39], s29 offen offset:1024 sc1\n"
      "s_waitcnt vmcnt(12)\n"
      "v_mfma_f32_32x32x16_bf16 v[40:55], v[132:135], v[220:223], v[40:55]\n"
      "ds_read_b64_tr_b16 v[96:97], v12 offset:51200\n"
      "ds_read_b64_tr_b16 v[98:99], v12 offset:52224\n"
      "ds_read_b64_tr_b16 v[100:101], v13 offset:51200\n"
      "v_mfma_f32_32x32x16_bf16 v[24:39], v[128:131], v[220:223], v[24:39]\n"
      "ds_read_b64_tr_b16 v[102:103], v13 offset:52224\n"
      "buffer_load_dwordx4 v[208:211], v16, s[36:39], s29 offen offset:2048 sc1\n"
      "ds_read_b64_tr_b16 v[104:105], v12 offset:53248\n"
      "s_waitcnt vmcnt(12)\n"
      "v_mfma_f32_32x32x16_bf16 v[40:55], v[140:143], v[224:227], v[40:55]\n"
      "ds_read_b64_tr_b16 v[106:107], v12 offset:54272\n"
      "ds_read_b64_tr_b16 v[108:109], v13 offset:53248\n"
      "ds_read_b64_tr_b16 v[110:111], v13 offset:54272\n"
      "v_mfma_f32_32x32x16_bf16 v[24:39], v[136:139], v[224:227], v[24:39]\n"
      "buffer_load_dwordx4 v[212:215], v16, s[36:39], s29 offen offset:3072 sc1\n"
      "ds_read_b64_tr_b16 v[112:113], v12 offset:55296\n"
      "ds_read_b64_tr_b16 v[114:115], v12 offset:56320\n"
      "s_waitcnt vmcnt(12)\n"
      "v_mfma_f32_32x32x16_bf16 v[40:55], v[148:151], v[228:231], v[40:55]\n"
      "ds_read_b64_tr_b16 v[116:117], v13 offset:55296\n"
      "s_waitcnt lgkmcnt(7)\n"
      "ds_read_b64_tr_b16 v[118:119], v13 offset:56320\n"
      "v_mfma_f32_32x32x16_bf16 v[24:39], v[144:147], v[228:231], v[24:39]\n"
      "s_waitcnt vmcnt(11)\n"
      "v_mfma_f32_32x32x16_bf16 v[40:55], v[156:159], v[232:235], v[40:55]\n"
      "buffer_load_dwordx4 v[216:219], v16, s[36:39], s30 offen offset:0 sc1\n"
      "ds_read_b64_tr_b16 v[120:121], v10 offset:0\n"
      "ds_read_b64_tr_b16 v[122:123], v10 offset:1024\n"
      "v_mfma_f32_32x32x16_bf16 v[24:39], v[152:155], v[232:235], v[24:39]\n"
      "ds_read_b64_tr_b16 v[124:125], v11 offset:0\n"
      "ds_read_b64_tr_b16 v[126:127], v11 offset:1024\n"
      "buffer_load_dwordx4 v[220:223], v16, s[36:39], s30 offen offset:1024 sc1\n"
      "s_waitcnt vmcnt(12)\n"
      "v_mfma_f32_32x32x16_bf16 v[40:55], v[164:167], v[236:239], v[40:55]\n"
      "ds_read_b64_tr_b16 v[128:129], v10 offset:2048\n"
      "ds_read_b64_tr_b16 v[130:131], v10 offset:3072\n"
      "ds_read_b64_tr_b16 v[132:133], v11 offset:2048\n"
      "v_mfma_f32_32x32x16_bf16 v[24:39], v[160:163], v[236:239], v[24:39]\n"
      "s_waitcnt lgkmcnt(7)\n"
      "ds_read_b64_tr_b16 v[134:135], v11 offset:3072\n"
      "buffer_load_dwordx4 v[224:227], v16, s[36:39], s30 offen offset:2048 sc1\n"
      "ds_read_b64_tr_b16 v[136:137], v10 offset:4096\n"
      "s_waitcnt vmcnt(12)\n"
      "v_mfma_f32_32x32x16_bf16 v[40:55], v[172:175], v[240:243], v[40:55]\n"
      "ds_read_b64_tr_b16 v[138:139], v10 offset:5120\n"
      "ds_read_b64_tr_b16 v[140:141], v11 offset:4096\n"
      "ds_read_b64_tr_b16 v[142:143], v11 offset:5120\n"
      "v_mfma_f32_32x32x16_bf16 v[24:39], v[168:171], v[240:243], v[24:39]\n"
      "buffer_load_dwordx4 v[228:231], v16, s[36:39], s30 offen offset:3072 sc1\n"
      "ds_read_b64_tr_b16 v[144:145], v10 offset:6144\n"
      "ds_read_b64_tr_b16 v[146:147], v10 offset:7168\n"
      "s_waitcnt vmcnt(12)\n"
      "v_mfma_f32_32x32x16_bf16 v[40:55], v[180:183], v[244:247], v[40:55]\n"
      "ds_read_b64_tr_b16 v[148:149], v11 offset:6144\n"
      "s_waitcnt lgkmcnt(7)\n"
      "ds_read_b64_tr_b16 v[150:151], v11 offset:7168\n"
      "v_mfma_f32_32x32x16_bf16 v[24:39], v[176:179], v[244:247], v[24:39]\n"
      "s_add_u32 s24, s24, 4096\n"
      "s_waitcnt vmcnt(11)\n"
      "v_mfma_f32_32x32x16_bf16 v[40:55], v[60:63], v[184:187], v[40:55]\n"
      "buffer_load_dwordx4 v[232:235], v16, s[36:39], s24 offen offset:0 sc1\n"
      "ds_read_b64_tr_b16 v[152:153], v10 offset:16384\n"
      "ds_read_b64_tr_b16 v[154:155], v10 offset:17408\n"
      "v_mfma_f32_32x32x16_bf16 v[24:39], v[56:59], v[184:187], v[24:39]\n"
      "ds_read_b64_tr_b16 v[156:157], v11 offset:16384\n"
      "ds_read_b64_tr_b16 v[158:159], v11 offset:17408\n"
      "buffer_load_dwordx4 v[236:239], v16, s[36:39], s24 offen offset:1024 sc1\n"
      "s_waitcnt vmcnt(12)\n"
      "v_mfma_f32_32x32x16_bf16 v[40:55], v[68:71], v[188:191], v[40:55]\n"
      "ds_read_b64_tr_b16 v[160:161], v10 offset:18432\n"
      "ds_read_b64_tr_b16 v[162:163], v10 offset:19456\n"
      "ds_read_b64_tr_b16 v[164:165], v11 offset:18432\n"
      "v_mfma_f32_32x32x16_bf16 v[24:39], v[64:67], v[188:191], v[24:39]\n"
      "s_waitcnt lgkmcnt(7)\n"
      "ds_read_b64_tr_b16 v[166:167], v11 offset:19456\n"
      "buffer_load_dwordx4 v[240:243], v16, s[36:39], s24 offen offset:2048 sc1\n"
      "ds_read_b64_tr_b16 v[168:169], v10 offset:20480\n"
      "s_waitcnt vmcnt(12)\n"
      "v_mfma_f32_32x32x16_bf16 v[40:55], v[76:79], v[192:195], v[40:55]\n"
      "ds_read_b64_tr_b16 v[170:171], v10 offset:21504\n"
      "ds_read_b64_tr_b16 v[172:173], v11 offset:20480\n"
      "ds_read_b64_tr_b16 v[174:175], v11 offset:21504\n"
      "v_mfma_f32_32x32x16_bf16 v[24:39], v[72:75], v[192:195], v[24:39]\n"
      "buffer_load_dwordx4 v[244:247], v16, s[36:39], s24 offen offset:3072 sc1\n"
      "ds_read_b64_tr_b16 v[176:177], v10 offset:22528\n"
      "ds_read_b64_tr_b16 v[178:179], v10 offset:23552\n"
      "s_waitcnt vmcnt(12)\n"
      "v_mfma_f32_32x32x16_bf16 v[40:55], v[84:87], v[196:199], v[40:55]\n"
      "ds_read_b64_tr_b16 v[180:181], v11 offset:22528\n"
      "s_waitcnt lgkmcnt(7)\n"
      "ds_read_b64_tr_b16 v[182:183], v11 offset:23552\n"
      "v_mfma_f32_32x32x16_bf16 v[24:39], v[80:83], v[196:199], v[24:39]\n"
      "s_add_u32 s25, s25, 4096\n"
      "s_waitcnt vmcnt(11)\n"
      "v_mfma_f32_32x32x16_bf16 v[40:55], v[92:95], v[200:203], v[40:55]\n"
      "buffer_load_dwordx4 v[184:187], v16, s[36:39], s25 offen offset:0 sc1\n"
      "ds_read_b64_tr_b16 v[56:57], v10 offset:32768\n"
      "ds_read_b64_tr_b16 v[58:59], v10 offset:33792\n"
      "v_mfma_f32_32x32x16_bf16 v[24:39], v[88:91], v[200:203], v[24:39]\n"
      "ds_read_b64_tr_b16 v[60:61], v11 offset:32768\n"
      "ds_read_b64_tr_b16 v[62:63], v11 offset:33792\n"
      "buffer_load_dwordx4 v[188:191], v16, s[36:39], s25 offen offset:1024 sc1\n"
      "s_waitcnt vmcnt(12)\n"
      "v_mfma_f32_32x32x16_bf16 v[40:55], v[100:103], v[204:207], v[40:55]\n"
      "ds_read_b64_tr_b16 v[64:65], v10 offset:34816\n"
      "ds_read_b64_tr_b16 v[66:67], v10 offset:35840\n"
      "ds_read_b64_tr_b16 v[68:69], v11 offset:34816\n"
      "v_mfma_f32_32x32x16_bf16 v[24:39], v[96:99], v[204:207], v[24:39]\n"
      "s_waitcnt lgkmcnt(7)\n"
      "ds_read_b64_tr_b16 v[70:71], v11 offset:35840\n"
      "buffer_load_dwordx4 v[192:195], v16, s[36:39], s25 offen offset:2048 sc1\n"
      "ds_read_b64_tr_b16 v[72:73], v10 offset:36864\n"
      "s_waitcnt vmcnt(12)\n"
      "v_mfma_f32_32x32x16_bf16 v[40:55], v[108:111], v[208:211], v[40:55]\n"
      "ds_read_b64_tr_b16 v[74:75], v10 offset:37888\n"
      "ds_read_b64_tr_b16 v[76:77], v11 offset:36864\n"
      "ds_read_b64_tr_b16 v[78:79], v11 offset:37888\n"
      "v_mfma_f32_32x32x16_bf16 v[24:39], v[104:107], v[208:211], v[24:39]\n"
      "buffer_load_dwordx4 v[196:199], v16, s[36:39], s25 offen offset:3072 sc1\n"
      "ds_read_b64_tr_b16 v[80:81], v10 offset:38912\n"
      "ds_read_b64_tr_b16 v[82:83], v10 offset:39936\n"
      "s_waitcnt vmcnt(12)\n"
      "v_mfma_f32_32x32x16_bf16 v[40:55], v[116:119], v[212:215], v[40:55]\n"
      "ds_read_b64_tr_b16 v[84:85], v11 offset:38912\n"
      "s_waitcnt lgkmcnt(7)\n"
      "ds_read_b64_tr_b16 v[86:87], v11 offset:39936\n"
      "v_mfma_f32_32x32x16_bf16 v[24:39], v[112:115], v[212:215], v[24:39]\n"
      "s_add_u32 s26, s26, 4096\n"
      "s_waitcnt vmcnt(11)\n"
      "v_mfma_f32_32x32x16_bf16 v[40:55], v[124:127], v[216:219], v[40:55]\n"
      "buffer_load_dwordx4 v[200:203], v16, s[36:39], s26 offen offset:0 sc1\n"
      "ds_read_b64_tr_b16 v[88:89], v10 offset:49152\n"
      "ds_read_b64_tr_b16 v[90:91], v10 offset:50176\n"
      "v_mfma_f32_32x32x16_bf16 v[24:39], v[120:123], v[216:219], v[24:39]\n"
      "ds_read_b64_tr_b16 v[92:93], v11 offset:49152\n"
      "ds_read_b64_tr_b16 v[94:95], v11 offset:50176\n"
      "buffer_load_dwordx4 v[204:207], v16, s[36:39], s26 offen offset:1024 sc1\n"
      "s_waitcnt vmcnt(12)\n"
      "v_mfma_f32_32x32x16_bf16 v[40:55], v[132:135], v[220:223], v[40:55]\n"
      "ds_read_b64_tr_b16 v[96:97], v10 offset:51200\n"
      "ds_read_b64_tr_b16 v[98:99], v10 offset:52224\n"
      "ds_read_b64_tr_b16 v[100:101], v11 offset:51200\n"
      "v_mfma_f32_32x32x16_bf16 v[24:39], v[128:131], v[220:223], v[24:39]\n"
      "s_waitcnt lgkmcnt(7)\n"
      "ds_read_b64_tr_b16 v[102:103], v11 offset:52224\n"
      "buffer_load_dwordx4 v[208:211], v16, s[36:39], s26 offen offset:2048 sc1\n"
      "ds_read_b64_tr_b16 v[104:105], v10 offset:53248\n"
      "s_waitcnt vmcnt(12)\n"
      "v_mfma_f32_32x32x16_bf16 v[40:55], v[140:143], v[224:227], v[40:55]\n"
      "ds_read_b64_tr_b16 v[106:107], v10 offset:54272\n"
      "ds_read_b64_tr_b16 v[108:109], v11 offset:53248\n"
      "ds_read_b64_tr_b16 v[110:111], v11 offset:54272\n"
      "v_mfma_f32_32x32x16_bf16 v[24:39], v[136:139], v[224:227], v[24:39]\n"
      "buffer_load_dwordx4 v[212:215], v16, s[36:39], s26 offen offset:3072 sc1\n"
      "ds_read_b64_tr_b16 v[112:113], v10 offset:55296\n"
      "ds_read_b64_tr_b16 v[114:115], v10 offset:56320\n"
      "s_waitcnt vmcnt(12)\n"
      "v_mfma_f32_32x32x16_bf16 v[40:55], v[148:151], v[228:231], v[40:55]\n"
      "ds_read_b64_tr_b16 v[116:117], v11 offset:55296\n"
      "s_waitcnt lgkmcnt(7)\n"
      "ds_read_b64_tr_b16 v[118:119], v11 offset:56320\n"
      "v_mfma_f32_32x32x16_bf16 v[24:39], v[144:147], v[228:231], v[24:39]\n"
      "s_add_u32 s27, s27, 4096\n"
      "s_waitcnt vmcnt(11)\n"
      "v_mfma_f32_32x32x16_bf16 v[40:55], v[156:159], v[232:235], v[40:55]\n"
      "buffer_load_dwordx4 v[216:219], v16, s[36:39], s27 offen offset:0 sc1\n"
      "ds_read_b64_tr_b16 v[120:121], v14 offset:0\n"
      "ds_read_b64_tr_b16 v[122:123], v14 offset:1024\n"
      "v_mfma_f32_32x32x16_bf16 v[24:39], v[152:155], v[232:235], v[24:39]\n"
      "ds_read_b64_tr_b16 v[124:125], v15 offset:0\n"
      "ds_read_b64_tr_b16 v[126:127], v15 offset:1024\n"
      "buffer_load_dwordx4 v[220:223], v16, s[36:39], s27 offen offset:1024 sc1\n"
      "s_waitcnt vmcnt(12)\n"
      "v_mfma_f32_32x32x16_bf16 v[40:55], v[164:167], v[236:239], v[40:55]\n"
      "ds_read_b64_tr_b16 v[128:129], v14 offset:2048\n"
      "ds_read_b64_tr_b16 v[130:131], v14 offset:3072\n"
      "ds_read_b64_tr_b16 v[132:133], v15 offset:2048\n"
      "v_mfma_f32_32x32x16_bf16 v[24:39], v[160:163], v[236:239], v[24:39]\n"
      "s_waitcnt lgkmcnt(7)\n"
      "ds_read_b64_tr_b16 v[134:135], v15 offset:3072\n"
      "buffer_load_dwordx4 v[224:227], v16, s[36:39], s27 offen offset:2048 sc1\n"
      "ds_read_b64_tr_b16 v[136:137], v14 offset:4096\n"
      "s_waitcnt vmcnt(12)\n"
      "v_mfma_f32_32x32x16_bf16 v[40:55], v[172:175], v[240:243], v[40:55]\n"
      "ds_read_b64_tr_b16 v[138:139], v14 offset:5120\n"
      "ds_read_b64_tr_b16 v[140:141], v15 offset:4096\n"
      "ds_read_b64_tr_b16 v[142:143], v15 offset:5120\n"
      "v_mfma_f32_32x32x16_bf16 v[24:39], v[168:171], v[240:243], v[24:39]\n"
      "buffer_load_dwordx4 v[228:231], v16, s[36:39], s27 offen offset:3072 sc1\n"
      "ds_read_b64_tr_b16 v[144:145], v14 offset:6144\n"
      "ds_read_b64_tr_b16 v[146:147], v14 offset:7168\n"
      "s_waitcnt vmcnt(12)\n"
      "v_mfma_f32_32x32x16_bf16 v[40:55], v[180:183], v[244:247], v[40:55]\n"
      "ds_read_b64_tr_b16 v[148:149], v15 offset:6144\n"
      "s_waitcnt lgkmcnt(7)\n"
      "ds_read_b64_tr_b16 v[150:151], v15 offset:7168\n"
      "v_mfma_f32_32x32x16_bf16 v[24:39], v[176:179], v[244:247], v[24:39]\n"
      "s_add_u32 s28, s28, 4096\n"
      "s_waitcnt vmcnt(11)\n"
      "v_mfma_f32_32x32x16_bf16 v[40:55], v[60:63], v[184:187], v[40:55]\n"
      "buffer_load_dwordx4 v[232:235], v16, s[36:39], s28 offen offset:0 sc1\n"
      "ds_read_b64_tr_b16 v[152:153], v12 offset:0\n"
      "ds_read_b64_tr_b16 v[154:155], v12 offset:1024\n"
      "v_mfma_f32_32x32x16_bf16 v[24:39], v[56:59], v[184:187], v[24:39]\n"
      "ds_read_b64_tr_b16 v[156:157], v13 offset:0\n"
      "ds_read_b64_tr_b16 v[158:159], v13 offset:1024\n"
      "buffer_load_dwordx4 v[236:239], v16, s[36:39], s28 offen offset:1024 sc1\n"
      "s_waitcnt vmcnt(12)\n"
      "v_mfma_f32_32x32x16_bf16 v[40:55], v[68:71], v[188:191], v[40:55]\n"
      "ds_read_b64_tr_b16 v[160:161], v12 offset:2048\n"
      "ds_read_b64_tr_b16 v[162:163], v12 offset:3072\n"
      "ds_read_b64_tr_b16 v[164:165], v13 offset:2048\n"
      "v_mfma_f32_32x32x16_bf16 v[24:39], v[64:67], v[188:191], v[24:39]\n"
      "s_waitcnt lgkmcnt(7)\n"
      "ds_read_b64_tr_b16 v[166:167], v13 offset:3072\n"
      "buffer_load_dwordx4 v[240:243], v16, s[36:39], s28 offen offset:2048 sc1\n"
      "ds_read_b64_tr_b16 v[168:169], v12 offset:4096\n"
      "s_waitcnt vmcnt(12)\n"
      "v_mfma_f32_32x32x16_bf16 v[40:55], v[76:79], v[192:195], v[40:55]\n"
      "ds_read_b64_tr_b16 v[170:171], v12 offset:5120\n"
      "ds_read_b64_tr_b16 v[172:173], v13 offset:4096\n"
      "ds_read_b64_tr_b16 v[174:175], v13 offset:5120\n"
      "v_mfma_f32_32x32x16_bf16 v[24:39], v[72:75], v[192:195], v[24:39]\n"
      "buffer_load_dwordx4 v[244:247], v16, s[36:39], s28 offen offset:3072 sc1\n"
      "ds_read_b64_tr_b16 v[176:177], v12 offset:6144\n"
      "ds_read_b64_tr_b16 v[178:179], v12 offset:7168\n"
      "s_waitcnt vmcnt(12)\n"
      "v_mfma_f32_32x32x16_bf16 v[40:55], v[84:87], v[196:199], v[40:55]\n"
      "ds_read_b64_tr_b16 v[180:181], v13 offset:6144\n"
      "s_waitcnt lgkmcnt(7)\n"
      "ds_read_b64_tr_b16 v[182:183], v13 offset:7168\n"
      "v_mfma_f32_32x32x16_bf16 v[24:39], v[80:83], v[196:199], v[24:39]\n"
      "s_add_u32 s29, s29, 4096\n"
      "s_waitcnt vmcnt(11)\n"
      "v_mfma_f32_32x32x16_bf16 v[40:55], v[92:95], v[200:203], v[40:55]\n"
      "buffer_load_dwordx4 v[184:187], v16, s[36:39], s29 offen offset:0 sc1\n"
      "ds_read_b64_tr_b16 v[56:57], v12 offset:16384\n"
      "ds_read_b64_tr_b16 v[58:59], v12 offset:17408\n"
      "v_mfma_f32_32x32x16_bf16 v[24:39], v[88:91], v[200:203], v[24:39]\n"
      "ds_read_b64_tr_b16 v[60:61], v13 offset:16384\n"
      "ds_read_b64_tr_b16 v[62:63], v13 offset:17408\n"
      "buffer_load_dwordx4 v[188:191], v16, s[36:39], s29 offen offset:1024 sc1\n"
      "s_waitcnt vmcnt(12)\n"
      "v_mfma_f32_32x32x16_bf16 v[40:55], v[100:103], v[204:207], v[40:55]\n"
      "ds_read_b64_tr_b16 v[64:65], v12 offset:18432\n"
      "ds_read_b64_tr_b16 v[66:67], v12 offset:19456\n"
      "ds_read_b64_tr_b16 v[68:69], v13 offset:18432\n"
      "v_mfma_f32_32x32x16_bf16 v[24:39], v[96:99], v[204:207], v[24:39]\n"
      "s_waitcnt lgkmcnt(7)\n"
      "ds_read_b64_tr_b16 v[70:71], v13 offset:19456\n"
      "buffer_load_dwordx4 v[192:195], v16, s[36:39], s29 offen offset:2048 sc1\n"
      "ds_read_b64_tr_b16 v[72:73], v12 offset:20480\n"
      "s_waitcnt vmcnt(12)\n"
      "v_mfma_f32_32x32x16_bf16 v[40:55], v[108:111], v[208:211], v[40:55]\n"
      "ds_read_b64_tr_b16 v[74:75], v12 offset:21504\n"
      "ds_read_b64_tr_b16 v[76:77], v13 offset:20480\n"
      "ds_read_b64_tr_b16 v[78:79], v13 offset:21504\n"
      "v_mfma_f32_32x32x16_bf16 v[24:39], v[104:107], v[208:211], v[24:39]\n"
      "buffer_load_dwordx4 v[196:199], v16, s[36:39], s29 offen offset:3072 sc1\n"
      "ds_read_b64_tr_b16 v[80:81], v12 offset:22528\n"
      "ds_read_b64_tr_b16 v[82:83], v12 offset:23552\n"
      "s_waitcnt vmcnt(12)\n"
      "v_mfma_f32_32x32x16_bf16 v[40:55], v[116:119], v[212:215], v[40:55]\n"
      "ds_read_b64_tr_b16 v[84:85], v13 offset:22528\n"
      "s_waitcnt lgkmcnt(7)\n"
      "ds_read_b64_tr_b16 v[86:87], v13 offset:23552\n"
      "v_mfma_f32_32x32x16_bf16 v[24:39], v[112:115], v[212:215], v[24:39]\n"
      "s_add_u32 s30, s30, 4096\n"
      "s_waitcnt vmcnt(11)\n"
      "v_mfma_f32_32x32x16_bf16 v[40:55], v[124:127], v[216:219], v[40:55]\n"
      "buffer_load_dwordx4 v[200:203], v16, s[36:39], s30 offen offset:0 sc1\n"
      "ds_read_b64_tr_b16 v[88:89], v14 offset:16384\n"
      "ds_read_b64_tr_b16 v[90:91], v14 offset:17408\n"
      "v_mfma_f32_32x32x16_bf16 v[24:39], v[120:123], v[216:219], v[24:39]\n"
      "ds_read_b64_tr_b16 v[92:93], v15 offset:16384\n"
      "ds_read_b64_tr_b16 v[94:95], v15 offset:17408\n"
      "buffer_load_dwordx4 v[204:207], v16, s[36:39], s30 offen offset:1024 sc1\n"
      "s_waitcnt vmcnt(12)\n"
      "v_mfma_f32_32x32x16_bf16 v[40:55], v[132:135], v[220:223], v[40:55]\n"
      "ds_read_b64_tr_b16 v[96:97], v14 offset:18432\n"
      "ds_read_b64_tr_b16 v[98:99], v14 offset:19456\n"
      "ds_read_b64_tr_b16 v[100:101], v15 offset:18432\n"
      "v_mfma_f32_32x32x16_bf16 v[24:39], v[128:131], v[220:223], v[24:39]\n"
      "s_waitcnt lgkmcnt(7)\n"
      "ds_read_b64_tr_b16 v[102:103], v15 offset:19456\n"
      "buffer_load_dwordx4 v[208:211], v16, s[36:39], s30 offen offset:2048 sc1\n"
      "ds_read_b64_tr_b16 v[104:105], v14 offset:20480\n"
      "s_waitcnt vmcnt(12)\n"
      "v_mfma_f32_32x32x16_bf16 v[40:55], v[140:143], v[224:227], v[40:55]\n"
      "ds_read_b64_tr_b16 v[106:107], v14 offset:21504\n"
      "ds_read_b64_tr_b16 v[108:109], v15 offset:20480\n"
      "ds_read_b64_tr_b16 v[110:111], v15 offset:21504\n"
      "v_mfma_f32_32x32x16_bf16 v[24:39], v[136:139], v[224:227], v[24:39]\n"
      "buffer_load_dwordx4 v[212:215], v16, s[36:39], s30 offen offset:3072 sc1\n"
      "ds_read_b64_tr_b16 v[112:113], v14 offset:22528\n"
      "ds_read_b64_tr_b16 v[114:115], v14 offset:23552\n"
      "s_waitcnt vmcnt(12)\n"
      "v_mfma_f32_32x32x16_bf16 v[40:55], v[148:151], v[228:231], v[40:55]\n"
      "ds_read_b64_tr_b16 v[116:117], v15 offset:22528\n"
      "s_waitcnt lgkmcnt(7)\n"
      "ds_read_b64_tr_b16 v[118:119], v15 offset:23552\n"
      "v_mfma_f32_32x32x16_bf16 v[24:39], v[144:147], v[228:231], v[24:39]\n"
      "s_waitcnt vmcnt(11)\n"
      "v_mfma_f32_32x32x16_bf16 v[40:55], v[156:159], v[232:235], v[40:55]\n"
      "v_mfma_f32_32x32x16_bf16 v[24:39], v[152:155], v[232:235], v[24:39]\n"
      "s_waitcnt vmcnt(10)\n"
      "v_mfma_f32_32x32x16_bf16 v[40:55], v[164:167], v[236:239], v[40:55]\n"
      "v_mfma_f32_32x32x16_bf16 v[24:39], v[160:163], v[236:239], v[24:39]\n"
      "s_waitcnt vmcnt(9)\n"
      "v_mfma_f32_32x32x16_bf16 v[40:55], v[172:175], v[240:243], v[40:55]\n"
      "v_mfma_f32_32x32x16_bf16 v[24:39], v[168:171], v[240:243], v[24:39]\n"
      "s_waitcnt vmcnt(8)\n"
      "v_mfma_f32_32x32x16_bf16 v[40:55], v[180:183], v[244:247], v[40:55]\n"
      "v_mfma_f32_32x32x16_bf16 v[24:39], v[176:179], v[244:247], v[24:39]\n"
      "s_waitcnt vmcnt(7)\n"
      "v_mfma_f32_32x32x16_bf16 v[40:55], v[60:63], v[184:187], v[40:55]\n"
      "v_mfma_f32_32x32x16_bf16 v[24:39], v[56:59], v[184:187], v[24:39]\n"
      "s_waitcnt vmcnt(6)\n"
      "v_mfma_f32_32x32x16_bf16 v[40:55], v[68:71], v[188:191], v[40:55]\n"
      "v_mfma_f32_32x32x16_bf16 v[24:39], v[64:67], v[188:191], v[24:39]\n"
      "s_waitcnt vmcnt(5)\n"
      "v_mfma_f32_32x32x16_bf16 v[40:55], v[76:79], v[192:195], v[40:55]\n"
      "v_mfma_f32_32x32x16_bf16 v[24:39], v[72:75], v[192:195], v[24:39]\n"
      "s_waitcnt vmcnt(4)\n"
      "v_mfma_f32_32x32x16_bf16 v[40:55], v[84:87], v[196:199], v[40:55]\n"
      "v_mfma_f32_32x32x16_bf16 v[24:39], v[80:83], v[196:199], v[24:39]\n"
      "s_waitcnt lgkmcnt(0)\n"
      "s_waitcnt vmcnt(3)\n"
      "v_mfma_f32_32x32x16_bf16 v[40:55], v[92:95], v[200:203], v[40:55]\n"
      "s_waitcnt vmcnt(2)\n"
      "v_mfma_f32_32x32x16_bf16 v[40:55], v[100:103], v[204:207], v[40:55]\n"
      "s_waitcnt vmcnt(1)\n"
      "v_mfma_f32_32x32x16_bf16 v[40:55], v[108:111], v[208:211], v[40:55]\n"
      "s_waitcnt vmcnt(0)\n"
      "v_mfma_f32_32x32x16_bf16 v[40:55], v[116:119], v[212:215], v[40:55]\n"
      "v_mfma_f32_32x32x16_bf16 v[24:39], v[88:91], v[200:203], v[24:39]\n"
      "s_nop 5\n"
      "v_mfma_f32_32x32x16_bf16 v[24:39], v[96:99], v[204:207], v[24:39]\n"
      "s_nop 3\n"
      "ds_write_b128 v4, v[40:43] offset:0\n"
      "v_mfma_f32_32x32x16_bf16 v[24:39], v[104:107], v[208:211], v[24:39]\n"
      "ds_write_b128 v4, v[44:47] offset:1024\n"
      "ds_write_b128 v4, v[48:51] offset:2048\n"
      "v_mfma_f32_32x32x16_bf16 v[24:39], v[112:115], v[212:215], v[24:39]\n"
      "ds_write_b128 v4, v[52:55] offset:3072\n"
      "s_waitcnt lgkmcnt(0)\n"
      "s_barrier\n"
      "ds_read_b128 v[56:59], v6 offset:0\n"
      "ds_read_b128 v[60:63], v6 offset:1024\n"
      "ds_read_b128 v[64:67], v6 offset:2048\n"
      "ds_read_b128 v[68:71], v6 offset:3072\n"
      "s_nop 4\n"
      "s_waitcnt lgkmcnt(3)\n"
      "v_pk_add_f32 v[24:25], v[24:25], v[56:57]\n"
      "v_pk_add_f32 v[26:27], v[26:27], v[58:59]\n"
      "s_waitcnt lgkmcnt(2)\n"
      "v_pk_add_f32 v[28:29], v[28:29], v[60:61]\n"
      "v_pk_add_f32 v[30:31], v[30:31], v[62:63]\n"
      "v_cvt_pk_bf16_f32 v240, v24, v25\n"
      "v_cvt_pk_bf16_f32 v241, v26, v27\n"
      "v_cvt_pk_bf16_f32 v242, v28, v29\n"
      "v_cvt_pk_bf16_f32 v243, v30, v31\n"
      "s_nop 1\n"
      "v_permlane32_swap_b32 v240, v242\n"
      "v_permlane32_swap_b32 v241, v243\n"
      "global_store_dwordx4 v3, v[240:243], s[18:19]\n"
      "s_waitcnt lgkmcnt(1)\n"
      "v_pk_add_f32 v[32:33], v[32:33], v[64:65]\n"
      "v_pk_add_f32 v[34:35], v[34:35], v[66:67]\n"
      "s_waitcnt lgkmcnt(0)\n"
      "v_pk_add_f32 v[36:37], v[36:37], v[68:69]\n"
      "v_pk_add_f32 v[38:39], v[38:39], v[70:71]\n"
      "v_cvt_pk_bf16_f32 v244, v32, v33\n"
      "v_cvt_pk_bf16_f32 v245, v34, v35\n"
      "v_cvt_pk_bf16_f32 v246, v36, v37\n"
      "v_cvt_pk_bf16_f32 v247, v38, v39\n"
      "s_nop 1\n"
      "v_permlane32_swap_b32 v244, v246\n"
      "v_permlane32_swap_b32 v245, v247\n"
      "global_store_dwordx4 v3, v[244:247], s[18:19] offset:32\n"
      "s_endpgm\n"
      "LW_%=:\n"
      "s_getpc_b64 s[50:51]\n"
      "v_lshlrev_b32 v6, 7, %2\n"
      "v_min_u32 v6, 12288, v6\n"
      "s_sub_u32 s50, s50, 12416\n"
      "s_subb_u32 s51, s51, 0\n"
      "s_sub_u32 s40, %1, 224\n"
      "s_lshr_b32 s40, s40, 3\n"
      "global_load_dword v20, v6, s[50:51]\n"
      "v_lshrrev_b32 v4, 3, %2\n"
      "v_and_b32 v5, 7, %2\n"
      "v_min_u32 v5, 6, v5\n"
      "v_lshlrev_b32 v5, 7, v5\n"
      "v_mul_u32_u24 v12, 896, v4\n"
      "v_add_u32 v12, v12, v5\n"
      "s_bfe_u32 s44, %1, 0x10002\n"
      "s_mul_i32 s44, s44, 1792\n"
      "v_mul_u32_u24 v11, 3584, v4\n"
      "v_lshl_add_u32 v11, v5, 1, v11\n"
      "v_add_u32 v11, s44, v11\n"
      "s_and_b32 s44, %1, 3\n"
      "s_lshl_b32 s44, s44, 9\n"
      "v_add_u32 v10, s44, %2\n"
      "v_mul_u32_u24 v10, 896, v10\n"
      "s_waitcnt lgkmcnt(0)\n"
      "s_sleep 15\n"
      "s_and_b32 s48, %1, 3\n"
      "s_mul_i32 s48, s48, 2\n"
      "s_mul_i32 s49, s48, 37\n"
      "s_lshr_b32 s49, s49, 8\n"
      "s_mul_i32 s49, s49, 7\n"
      "s_sub_u32 s48, s48, s49\n"
      "s_and_b32 s45, %1, 3\n"
      "s_bfe_u32 s49, %1, 0x10002\n"
      "s_mul_i32 s49, s49, 4\n"
      "s_add_u32 s45, s45, s49\n"
      "s_mul_i32 s49, s45, 37\n"
      "s_lshr_b32 s49, s49, 8\n"
      "s_mul_i32 s49, s49, 7\n"
      "s_sub_u32 s45, s45, s49\n"
      "s_lshl_b32 s45, s45, 1\n"
      "s_add_u32 s41, s40, 0\n"
      "s_add_u32 s41, s41, s45\n"
      "s_sub_u32 s42, s41, 14\n"
      "s_min_u32 s41, s41, s42\n"
      "s_mul_i32 s41, s41, 114688\n"
      "v_add_u32 v24, s41, v12\n"
      "global_load_dword v20, v24, s[14:15]\n"
      "s_add_u32 s41, s40, 0\n"
      "s_add_u32 s41, s41, s45\n"
      "s_sub_u32 s42, s41, 14\n"
      "s_min_u32 s41, s41, s42\n"
      "s_mul_i32 s41, s41, 114688\n"
      "s_add_u32 s41, s41, 57344\n"
      "v_add_u32 v25, s41, v12\n"
      "global_load_dword v20, v25, s[14:15]\n"
      "s_add_u32 s41, s40, 4\n"
      "s_add_u32 s41, s41, s45\n"
      "s_sub_u32 s42, s41, 14\n"
      "s_min_u32 s41, s41, s42\n"
      "s_mul_i32 s41, s41, 114688\n"
      "v_add_u32 v26, s41, v12\n"
      "global_load_dword v20, v26, s[14:15]\n"
      "s_add_u32 s41, s40, 8\n"
      "s_add_u32 s41, s41, s45\n"
      "s_sub_u32 s42, s41, 14\n"
      "s_min_u32 s41, s41, s42\n"
      "s_mul_i32 s41, s41, 114688\n"
      "v_add_u32 v27, s41, v12\n"
      "global_load_dword v20, v27, s[14:15]\n"
      "s_add_u32 s41, s40, 12\n"
      "s_add_u32 s41, s41, s45\n"
      "s_sub_u32 s42, s41, 14\n"
      "s_min_u32 s41, s41, s42\n"
      "s_mul_i32 s41, s41, 114688\n"
      "v_add_u32 v28, s41, v12\n"
      "s_sub_u32 s41, s40, 0\n"
      "s_cmp_ge_u32 s41, 2\n"
      "s_cbranch_scc1 LX28_%=\n"
      "global_load_dword v20, v28, s[14:15]\n"
      "LX28_%=:\n"
      "s_add_u32 s41, s40, 2\n"
      "s_add_u32 s41, s41, s45\n"
      "s_sub_u32 s42, s41, 14\n"
      "s_min_u32 s41, s41, s42\n"
      "s_mul_i32 s41, s41, 114688\n"
      "s_add_u32 s41, s41, 57344\n"
      "v_add_u32 v29, s41, v12\n"
      "s_sub_u32 s41, s40, 2\n"
      "s_cmp_ge_u32 s41, 2\n"
      "s_cbranch_scc1 LX29_%=\n"
      "global_load_dword v20, v29, s[14:15]\n"
      "LX29_%=:\n"
      "s_add_u32 s41, s40, 6\n"
      "s_add_u32 s41, s41, s45\n"
      "s_sub_u32 s42, s41, 14\n"
      "s_min_u32 s41, s41, s42\n"
      "s_mul_i32 s41, s41, 114688\n"
      "s_add_u32 s41, s41, 57344\n"
      "v_add_u32 v30, s41, v12\n"
      "global_load_dword v20, v30, s[14:15]\n"
      "s_add_u32 s41, s40, 10\n"
      "s_add_u32 s41, s41, s45\n"
      "s_sub_u32 s42, s41, 14\n"
      "s_min_u32 s41, s41, s42\n"
      "s_mul_i32 s41, s41, 114688\n"
      "s_add_u32 s41, s41, 57344\n"
      "v_add_u32 v31, s41, v12\n"
      "global_load_dword v20, v31, s[14:15]\n"
      "s_waitcnt vmcnt(0)\n"
      "s_endpgm\n"
      "LEND_%=:\n"
      :
      : "s"(__builtin_amdgcn_kernarg_segment_ptr()), "s"(blockIdx.x), "v"(threadIdx.x)
      : "memory", "vcc", "scc", "v1", "v2", "v3", "v4", "v5", "v6", "v7", "v8", "v9", "v10", "v11", "v12", "v13", "v14", "v15", "v16",
        "v17", "v18", "v19", "v20", "v21", "v22", "v23", "v24", "v25", "v26", "v27", "v28", "v29", "v30", "v31", "v32",
        "v33", "v34", "v35", "v36", "v37", "v38", "v39", "v40", "v41", "v42", "v43", "v44", "v45", "v46", "v47", "v48",
        "v49", "v50", "v51", "v52", "v53", "v54", "v55", "v56", "v57", "v58", "v59", "v60", "v61", "v62", "v63", "v64",
        "v65", "v66", "v67", "v68", "v69", "v70", "v71", "v72", "v73", "v74", "v75", "v76", "v77", "v78", "v79", "v80",
        "v81", "v82", "v83", "v84", "v85", "v86", "v87", "v88", "v89", "v90", "v91", "v92", "v93", "v94", "v95", "v96",
        "v97", "v98", "v99", "v100", "v101", "v102", "v103", "v104", "v105", "v106", "v107", "v108", "v109", "v110", "v111", "v112",
        "v113", "v114", "v115", "v116", "v117", "v118", "v119", "v120", "v121", "v122", "v123", "v124", "v125", "v126", "v127", "v128",
        "v129", "v130", "v131", "v132", "v133", "v134", "v135", "v136", "v137", "v138", "v139", "v140", "v141", "v142", "v143", "v144",
        "v145", "v146", "v147", "v148", "v149", "v150", "v151", "v152", "v153", "v154", "v155", "v156", "v157", "v158", "v159", "v160",
        "v161", "v162", "v163", "v164", "v165", "v166", "v167", "v168", "v169", "v170", "v171", "v172", "v173", "v174", "v175", "v176",
        "v177", "v178", "v179", "v180", "v181", "v182", "v183", "v184", "v185", "v186", "v187", "v188", "v189", "v190", "v191", "v192",
        "v193", "v194", "v195", "v196", "v197", "v198", "v199", "v200", "v201", "v202", "v203", "v204", "v205", "v206", "v207", "v208",
        "v209", "v210", "v211", "v212", "v213", "v214", "v215", "v216", "v217", "v218", "v219", "v220", "v221", "v222", "v223", "v224",
        "v225", "v226", "v227", "v228", "v229", "v230", "v231", "v232", "v233", "v234", "v235", "v236", "v237", "v238", "v239", "v240",
        "v241", "v242", "v243", "v244", "v245", "v246", "v247", "v248", "v249", "v250", "v251", "v252", "v253", "v254", "v255", "s4",
        "s5", "s6", "s7", "s8", "s9", "s10", "s11", "s12", "s13", "s14", "s15", "s16", "s17", "s18", "s19", "s20",
        "s21", "s22", "s23", "s24", "s25", "s26", "s27", "s28", "s29", "s30", "s31", "s36", "s37", "s38", "s39", "s40",
        "s41", "s42", "s43", "s44", "s45", "s46", "s47", "s48", "s49", "s50", "s51", "s52", "s53", "s54", "s55", "s56",
        "s57", "s58", "s59", "s60", "s61", "s62", "s63", "s64", "s65", "s66", "s67", "s68", "s69", "s70", "s71", "s72",
        "s73", "s74", "s75", "s76", "s77", "s78", "s79", "s80", "s81", "s82", "s83", "s84", "s85", "s86", "s87", "s88",
        "s89", "s90", "s91", "s92", "s93", "s94", "s95", "s96", "s97", "s98", "s99", "s100", "s101");
}

extern "C" void kernel_launch(void* const* d_in, const int* in_sizes, int n_in, void* d_out, int out_size,
                              void* d_ws, size_t ws_size, hipStream_t stream) {
  (void)hipFuncSetAttribute((const void*)fused_mlp, hipFuncAttributeMaxDynamicSharedMemorySize, 163840);
  fused_mlp<<<dim3(256), dim3(512), 163840, stream>>>((const u16*)d_in[0], (const u16*)d_in[1], (const u16*)d_in[2],
                                                  (const u16*)d_in[3], (const u16*)d_in[4], (u16*)d_out, (char*)d_ws);
}
```
